```python
import torch

def _softmax(x, axis=-1):
    return torch.softmax(x, dim=axis)
def _gelu(x):
    return torch.nn.functional.gelu(x, approximate="tanh")
def _silu(x):
    return torch.nn.functional.silu(x)
def _sigmoid(x):
    return torch.sigmoid(x)
def _layernorm(x, axis=-1, eps=1e-5):
    m = x.mean(dim=axis, keepdim=True)
    v = x.var(dim=axis, keepdim=True, unbiased=False)
    return (x - m) / torch.sqrt(v + eps)
def _scatter_add(tgt, idx, src, axis=0):
    return tgt.clone().index_add_(axis, idx, src)
def _segment_sum(src, seg, n):
    out = torch.zeros((n,) + src.shape[1:], dtype=src.dtype)
    return out.index_add_(0, seg, src)
def _pad(x, pw):
    flat = tuple(v for pair in reversed(pw) for v in pair)
    return torch.nn.functional.pad(x, flat)

kernel_name = 'interleave_layers__linear_kernel_hedgehog__mixer_s_mlp__prompt'

def setup_inputs():

    _w = (lambda a: (lambda t: t.to(torch.bfloat16) if t.is_floating_point() else t)(torch.as_tensor(a)))
    def _A_setup():
        B, S, D, H = 1, 2048, 512, 32
        Dh = D // H
        return {
            "x": _w((lambda *s: torch.randn(*s, dtype=torch.bfloat16) * 0.05)(B, S, D)),
            "wqkv": _w((lambda *s: torch.randn(*s, dtype=torch.bfloat16) * 0.4)(D, 3 * D)),
            "w_phi": _w((lambda *s: torch.randn(*s, dtype=torch.bfloat16) * 0.4)(H, Dh, Dh) * (Dh ** -0.5)),
            "wo": _w((lambda *s: torch.randn(*s, dtype=torch.bfloat16) * 0.05)(D, D)),
        }
    _A_in = _A_setup()

    def _B_setup():
        B, S, D, F = 1, 2048, 512, 1792
        return {
            "x":  _w((lambda *s: torch.randn(*s, dtype=torch.bfloat16) * 0.05)(B, S, D)),
            "w1": _w((lambda *s: torch.randn(*s, dtype=torch.bfloat16) * 3.2)(D, D) * (D ** -0.5)),
            "w2": _w((lambda *s: torch.randn(*s, dtype=torch.bfloat16) * 3.2)(D, D) * (D ** -0.5)),
            "w3": _w((lambda *s: torch.randn(*s, dtype=torch.bfloat16) * 3.2)(D, F) * (D ** -0.5)),
            "w4": _w((lambda *s: torch.randn(*s, dtype=torch.bfloat16) * 3.2)(F, D) * (F ** -0.5)),
        }
    _B_in = _B_setup()

    return {
        "A_x": _A_in["x"],
        "A_wqkv": _A_in["wqkv"],
        "A_w_phi": _A_in["w_phi"],
        "A_wo": _A_in["wo"],
        "B_x": _B_in["x"],
        "B_w1": _B_in["w1"],
        "B_w2": _B_in["w2"],
        "B_w3": _B_in["w3"],
        "B_w4": _B_in["w4"],
    }

def reference(A_x, A_wqkv, A_w_phi, A_wo, B_x, B_w1, B_w2, B_w3, B_w4):
    def _A_ref(x, wqkv, w_phi, wo):
        B, S, D = x.shape
        H = 32; Dh = D // H
        qkv = (lambda x, *d: x.permute(*d))((x @ wqkv).reshape(B, S, 3, H, Dh), 2, 0, 3, 1, 4)
        q, k, v = qkv[0], qkv[1], qkv[2]
        pq = torch.einsum("bhsd,hdf->bhsf", q, w_phi)
        pk = torch.einsum("bhsd,hdf->bhsf", k, w_phi)
        phi_q = torch.concatenate([_softmax(pq, axis=-1), _softmax(-pq, axis=-1)], axis=-1)
        phi_k = torch.concatenate([_softmax(pk, axis=-1), _softmax(-pk, axis=-1)], axis=-1)
        kv = torch.einsum("bhsf,bhsd->bhfd", phi_k, v)
        z  = torch.einsum("bhsf->bhf", phi_k)
        num = torch.einsum("bhqf,bhfd->bhqd", phi_q, kv)
        den = torch.einsum("bhqf,bhf->bhq", phi_q, z)[..., None] + 1e-6
        o = num / den
        return (lambda x, *d: x.permute(*d))(o, 0, 2, 1, 3).reshape(B, S, D) @ wo

    def _B_ref(x, w1, w2, w3, w4):
        B, S, D = x.shape
        h = _gelu(_layernorm(x, axis=-1) @ w1)
        c = D // 4
        g0 = _pad(h[:, :, 0*c:1*c], ((0, 0), (1, 0), (0, 0)))[:, :S, :]
        g1 = _pad(h[:, :, 1*c:2*c], ((0, 0), (0, 1), (0, 0)))[:, 1:, :]
        g2 = _pad(h[:, :, 2*c:3*c], ((0, 0), (2, 0), (0, 0)))[:, :S, :]
        g3 = _pad(h[:, :, 3*c:   ], ((0, 0), (0, 2), (0, 0)))[:, 2:, :]
        hs = torch.concatenate([g0, g1, g2, g3], axis=-1) @ w2
        x1 = x + hs
        h2 = _gelu(_layernorm(x1, axis=-1) @ w3) @ w4
        return x1 + h2

    x = A_x
    x = x + _A_ref(_layernorm(x, axis=-1), A_wqkv, A_w_phi, A_wo)
    x = x + _B_ref(_layernorm(x, axis=-1), B_w1, B_w2, B_w3, B_w4)
    x = x + _A_ref(_layernorm(x, axis=-1), A_wqkv, A_w_phi, A_wo)
    x = x + _B_ref(_layernorm(x, axis=-1), B_w1, B_w2, B_w3, B_w4)
    return x
```

```cpp
#include <hip/hip_runtime.h>
#include <hip/hip_bf16.h>
#include <type_traits>

#define GAS __attribute__((address_space(1)))
#define LAS __attribute__((address_space(3)))
#define DI __device__ __forceinline__
#define RLX_AGENT __ATOMIC_RELAXED, __HIP_MEMORY_SCOPE_AGENT
#pragma clang diagnostic ignored "-Wint-to-pointer-cast"

typedef unsigned u32;
typedef unsigned long long u64;
typedef u32 u32x4 __attribute__((ext_vector_type(4)));
typedef u32 u32x2 __attribute__((ext_vector_type(2)));
typedef float f32x2 __attribute__((ext_vector_type(2)));
typedef float f32x4 __attribute__((ext_vector_type(4)));
typedef float f32x16 __attribute__((ext_vector_type(16)));
typedef __bf16 bf16x2 __attribute__((ext_vector_type(2)));
typedef __bf16 bf16x8 __attribute__((ext_vector_type(8)));
typedef short s16x4 __attribute__((ext_vector_type(4)));

namespace {

constexpr u32 WS_EPOCH = 0, WS_TMO = 128, WS_FLAGS = 4096;
constexpr u32 FL_X = 0, FL_KV = 64, FL_O = 96, FL_XA = 128, FL_H = 192, FL_X1 = 256, FL_H2 = 320, FL_PT = 384;
constexpr u32 SZ_ACT = 2048u * 512u * 2u, SZ_ST = 64u * 4096u, SZ_KVP = 256u * 4352u, SZ_H2 = 2048u * 1792u * 2u;
constexpr u32 O_X = 0, O_XST = O_X + SZ_ACT, O_KVP = O_XST + SZ_ST, O_O = O_KVP + SZ_KVP, O_XA = O_O + SZ_ACT,
              O_XAST = O_XA + SZ_ACT, O_H = O_XAST + SZ_ST, O_X1 = O_H + SZ_ACT, O_X1ST = O_X1 + SZ_ACT,
              O_H2 = O_X1ST + SZ_ST, SZ_LAYER = O_H2 + SZ_H2;
constexpr u32 WS_L0 = 65536, WS_W3T = WS_L0 + 2 * SZ_LAYER, SZ_WT = 512u * 1792u * 2u, WS_W4T = WS_W3T + SZ_WT,
              WS_DBG = WS_W4T + SZ_WT, WS_END = WS_DBG + 256u * 128u * 8u;
static_assert(SZ_LAYER % 128 == 0, "align");

constexpr u32 L_CS1 = 0;
constexpr u32 L_CS3 = 512;
constexpr u32 L_CS5 = 1024;
constexpr u32 L_STM = 2048;
constexpr u32 L_STR = 2304;
constexpr u32 L_WPHI = 2560;
constexpr u32 L_KVT = 4608;
constexpr u32 L_ZT = 6656;
constexpr u32 L_W = 8192;
constexpr u32 L_A = 98304;
constexpr u32 L_PK = 106496;
constexpr u32 L_V = 122880;
constexpr u32 L_PT = 131072;
constexpr u32 L_RED = 114688;
constexpr u32 LDS_BYTES = 163840;

constexpr float LN_EPS = 1e-5f;
constexpr float LOG2E = 1.4426950408889634f;
constexpr u32 SPIN_CAP = 1u << 17;
constexpr u32 ONES = 0x3f803f80u;

struct Params {
  const unsigned short *x, *wqkv, *wphi, *wo, *w1, *w2, *w3, *w4;
  unsigned short* out;
  unsigned char* ws;
};

template <int N, class F> DI void static_for(F&& f) {
  if constexpr (N > 0) { static_for<N - 1>(f); f(std::integral_constant<int, N - 1>{}); }
}
DI u32 opq_s(u32 x) { asm volatile("" : "+s"(x)); return x; }
DI u32 opq_v(u32 x) { asm volatile("" : "+v"(x)); return x; }
DI int fresh_lane() {
  int l;
  asm volatile("v_mbcnt_lo_u32_b32 %0, -1, 0\n\tv_mbcnt_hi_u32_b32 %0, -1, %0" : "=v"(l));
  return l;
}
DI u32 pk2(float a, float b) {
  f32x2 v = {a, b};
  return __builtin_bit_cast(u32, __builtin_convertvector(v, bf16x2));
}
DI float as_f(u32 x) { return __builtin_bit_cast(float, x); }
DI u32 as_u(float x) { return __builtin_bit_cast(u32, x); }
DI float bflo(u32 u) { return __builtin_bit_cast(float, u << 16); }
DI float bfhi(u32 u) { return __builtin_bit_cast(float, u & 0xffff0000u); }
DI f32x16 mfma32(u32x4 a, u32x4 b, f32x16 c) {
  return __builtin_amdgcn_mfma_f32_32x32x16_bf16(__builtin_bit_cast(bf16x8, a), __builtin_bit_cast(bf16x8, b), c, 0, 0, 0);
}
DI f32x4 mfma16(u32x4 a, u32x4 b, f32x4 c) {
  return __builtin_amdgcn_mfma_f32_16x16x32_bf16(__builtin_bit_cast(bf16x8, a), __builtin_bit_cast(bf16x8, b), c, 0, 0, 0);
}
DI u32x4 lds128(u32 a) { return *(LAS u32x4*)a; }
DI f32x4 lds128f(u32 a) { return *(LAS f32x4*)a; }
DI float ldsf(u32 a) { return *(LAS float*)a; }
DI void stsf(u32 a, float v) { *(LAS float*)a = v; }
DI void sts128(u32 a, u32x4 v) { *(LAS u32x4*)a = v; }
DI void sts128f(u32 a, f32x4 v) { *(LAS f32x4*)a = v; }
DI u32x4 trfrag(u32 a0, u32 a1) {
  s16x4 lo = __builtin_amdgcn_ds_read_tr16_b64_v4i16((LAS s16x4*)a0);
  s16x4 hi = __builtin_amdgcn_ds_read_tr16_b64_v4i16((LAS s16x4*)a1);
  return __builtin_bit_cast(u32x4, __builtin_shufflevector(lo, hi, 0, 1, 2, 3, 4, 5, 6, 7));
}
DI u32x4 mk_srd(const void* p, u32 bytes) {
  const u64 a = (u64)p;
  return u32x4{(u32)a, (u32)(a >> 32) & 0xffffu, bytes, 0x00020000u};
}
template <u32 SC, u32 LC> DI void dma_sc1(u32x4 srd, u32 voff, u32 sb, u32 lb) {
  u32 t;
  asm volatile("s_add_u32 m0, %1, %2\n\ts_add_u32 %0, %3, %4\n\tbuffer_load_dwordx4 %5, %6, %0 offen sc1 lds"
               : "=&s"(t) : "s"(lb), "i"(LC), "s"(sb), "i"(SC), "v"(voff), "s"(srd) : "memory", "scc");
}
template <u32 SC, u32 LC> DI void dma_pl(u32x4 srd, u32 voff, u32 sb, u32 lb) {
  u32 t;
  asm volatile("s_add_u32 m0, %1, %2\n\ts_add_u32 %0, %3, %4\n\tbuffer_load_dwordx4 %5, %6, %0 offen lds"
               : "=&s"(t) : "s"(lb), "i"(LC), "s"(sb), "i"(SC), "v"(voff), "s"(srd) : "memory", "scc");
}
template <int N> DI void wait_bar() { asm volatile("s_waitcnt vmcnt(%0) lgkmcnt(0)\n\ts_barrier" ::"i"(N) : "memory"); }
template <int N> DI void wait_vm() { asm volatile("s_waitcnt vmcnt(%0)" ::"i"(N) : "memory"); }
DI void cwait_vm0() { __builtin_amdgcn_s_waitcnt(0x0F70); }
#define SB() __builtin_amdgcn_sched_barrier(0)
#define PIN(x) asm volatile("" : "+v"(x))
DI void bar_lds() { asm volatile("s_waitcnt lgkmcnt(0)\n\ts_barrier" ::: "memory"); }
DI void undef_R(u32x4 (&R)[32]) {
#pragma unroll
  for (int i = 0; i < 32; ++i) asm volatile("" : "=v"(R[i]));
}
DI float xhalf_sum(float v) {
  auto r = __builtin_amdgcn_permlane32_swap(as_u(v), as_u(v), false, false);
  return as_f(r[0]) + as_f(r[1]);
}
DI float gelu(float x) {
  const float c1 = -2.3022081986f, c2 = -0.1029432397f;
  const float t = __builtin_fmaf(x * x, c2, c1);
  const float e = __builtin_amdgcn_exp2f(x * t);
  return x * __builtin_amdgcn_rcpf(1.0f + e);
}

struct Ctx {
  __amdgpu_buffer_rsrc_t rs;
  u32x4 srd;
  GAS unsigned char* ws;
  u64 tag;
  int w, g, j, b;
};

DI void poll(const Ctx& c, u32 line_of_lane, u32 slot, bool active) {
  const GAS u64* p = (const GAS u64*)(c.ws + WS_FLAGS + line_of_lane * 128u + slot * 8u);
  GAS u32* tmo = (GAS u32*)(c.ws + WS_TMO);
  for (u32 spins = 1;; ++spins) {
    u64 v = c.tag;
    if (active) v = __hip_atomic_load(p, RLX_AGENT);
    if (__all(v == c.tag)) break;
    if ((spins & 63u) == 0u) {
      if (__builtin_amdgcn_readfirstlane(__hip_atomic_load(tmo, RLX_AGENT)) == (u32)c.tag) break;
      if (spins > SPIN_CAP) { __hip_atomic_store(tmo, (u32)c.tag, RLX_AGENT); break; }
    }
  }
  __builtin_amdgcn_fence(__ATOMIC_ACQUIRE, "wavefront");
}
DI void set_flag(const Ctx& c, u32 line, u32 slot) {
  if (c.w == 0) {
    if (fresh_lane() == 0) __hip_atomic_store((GAS u64*)(c.ws + WS_FLAGS + line * 128u + slot * 8u), c.tag, RLX_AGENT);
  }
}
DI void st128(const Ctx& c, u32x4 v, u32 voff, u32 soff) { __builtin_amdgcn_raw_buffer_store_b128(v, c.rs, voff, soff, 16); }
DI u32x4 ld128(const Ctx& c, u32 voff, u32 soff) { return __builtin_amdgcn_raw_buffer_load_b128(c.rs, voff, soff, 16); }

DI void pack2(const f32x16& v, u32x4& f0, u32x4& f1) {
  f0 = u32x4{pk2(v[0], v[1]), pk2(v[2], v[3]), pk2(v[4], v[5]), pk2(v[6], v[7])};
  f1 = u32x4{pk2(v[8], v[9]), pk2(v[10], v[11]), pk2(v[12], v[13]), pk2(v[14], v[15])};
}
DI void publish_tile(const Ctx& c, int lane, const f32x16& v, u32 soff) {
  u32x4 f0, f1;
  pack2(v, f0, f1);
  st128(c, f0, lane * 16, soff);
  st128(c, f1, lane * 16 + 1024, soff);
}
DI void publish_stats(const Ctx& c, int lane, const f32x16& v, u32 soff) {
  float s = 0.f, q = 0.f;
#pragma unroll
  for (int i = 0; i < 16; ++i) { s += v[i]; q = __builtin_fmaf(v[i], v[i], q); }
  s = xhalf_sum(s); q = xhalf_sum(q);
  const u32x2 o = {as_u(s), as_u(q)};
  if (lane < 32) __builtin_amdgcn_raw_buffer_store_b64(o, c.rs, lane * 8, soff, 16);
}
struct Stat8 { u32x2 v[8]; };
DI void load_stats(const Ctx& c, int lane, Stat8& st, u32 soff) {
  const u32 vo = (lane >> 5) * 2048 + (lane & 31) * 8;
#pragma unroll
  for (int i = 0; i < 8; ++i) st.v[i] = __builtin_amdgcn_raw_buffer_load_b64(c.rs, vo + i * 256, soff, 16);
}
DI void finish_stats(const Stat8& st, float& mean, float& var) {
  float s = 0.f, q = 0.f;
#pragma unroll
  for (int i = 0; i < 8; ++i) { s += as_f(st.v[i].x); q += as_f(st.v[i].y); }
  s = xhalf_sum(s); q = xhalf_sum(q);
  mean = s * (1.0f / 512.0f);
  var = __builtin_fmaxf(__builtin_fmaf(-mean, mean, q * (1.0f / 512.0f)), 0.f);
}
DI f32x16 load_cs(u32 base, int h) {
  f32x16 r;
#pragma unroll
  for (int q = 0; q < 4; ++q) {
    const f32x4 t = lds128f(base + h * 16 + 32 * q);
    r[4 * q] = t[0]; r[4 * q + 1] = t[1]; r[4 * q + 2] = t[2]; r[4 * q + 3] = t[3];
  }
  return r;
}
DI void store_cs(u32 base, const f32x16& v, int lane) {
  if ((lane & 31) == 0) {
#pragma unroll
    for (int q = 0; q < 4; ++q) sts128f(base + (lane >> 5) * 16 + 32 * q, f32x4{v[4 * q], v[4 * q + 1], v[4 * q + 2], v[4 * q + 3]});
  }
}

#define STAMP(i) do { } while (0)

DI void dma_w_small(const Ctx& c, const unsigned short* wp) {
  const int lane = fresh_lane();
  const u32x4 srd = mk_srd(wp, 512u * 512u * 2u);
  const int r = lane >> 3;
  const u32 voff = r * 1024 + (((lane & 7) ^ ((r & 2) << 1)) * 16) + 128 * c.j;
  const u32 sb = opq_s((c.w - 4) * 16 * 8192), lb = opq_s(L_W + (c.w - 4) * 16 * 1024);
  static_for<16>([&](auto I) { constexpr int i = I; dma_pl<i * 8192, i * 1024>(srd, voff, sb, lb); });
}
DI void dma_w1(const Ctx& c, const unsigned short* wp) {
  const int lane = fresh_lane();
  const u32x4 srd = mk_srd(wp, 512u * 1536u * 2u);
  const u32 voff = (lane >> 2) * 3072 + (lane & 3) * 16 + 64 * (c.j + 8 * (c.g & 1));
  const u32 sb = opq_s((c.w - 4) * 8 * 49152), lb = opq_s(L_W + (c.w - 4) * 8 * 1024);
  static_for<24>([&](auto I) {
    constexpr int i = I, sec = i / 8, n = i % 8;
    dma_pl<n * 49152 + sec * 1024, sec * 32768 + n * 1024>(srd, voff, sb, lb);
  });
}

template <bool SHIFT>
DI void small_issue(const Ctx& c, int lane, u32 abase) {
  const int w = c.w;
  const u32 lb = opq_s(L_A + w * 2048);
  if constexpr (SHIFT) {
    const u32 sb = opq_s(abase + w * 1024);
    static_for<4>([&](auto Q) {
      constexpr int q = Q, d = q == 0 ? -1 : q == 1 ? 1 : q == 2 ? -2 : 2;
      static_for<2>([&](auto T) {
        constexpr int t = T;
        const int tau = 64 * c.g + 32 * t + (lane & 31) + d;
        const u32 vo = (tau < 0 || tau >= 2048) ? 0x80000000u : (u32)((tau >> 5) * 32768 + ((tau & 31) + (lane & 32)) * 16);
        dma_sc1<q * 8192, q * 16384 + t * 1024>(c.srd, vo, sb, lb);
      });
    });
  } else {
    const u32 sb = opq_s(abase + 2 * c.g * 32768 + w * 1024);
    const u32 vo = lane * 16;
    static_for<4>([&](auto Q) {
      constexpr int q = Q;
      static_for<2>([&](auto T) { constexpr int t = T; dma_sc1<t * 32768 + q * 8192, q * 16384 + t * 1024>(c.srd, vo, sb, lb); });
    });
  }
}
DI u32 small_wb(int lane, int cc) {
  const int gq = lane >> 4, q4 = (lane & 15) >> 2, p = lane & 3;
  return L_W + (4 * (gq >> 1) + q4) * 128 + (((4 * cc + 2 * (gq & 1) + (p >> 1)) ^ ((q4 & 2) << 1)) * 16) + 8 * (p & 1);
}
DI f32x16 small_compute(const Ctx& c, int lane) {
  const int w = c.w;
  const u32 wb = opq_v(small_wb(lane, w & 1));
  const u32 ab = opq_v(L_A + (w >> 1) * 1024 + lane * 16);
  f32x16 acc = {};
  u32x4 wf[32], xf[32];
  constexpr int PD = 3;
  static_for<PD>([&](auto K) { constexpr int ks = K; wf[ks] = trfrag(wb + ks * 2048, wb + ks * 2048 + 1024); });
  SB();
  static_for<4>([&](auto Q) {
    constexpr int q = Q;
    wait_bar<6 - 2 * q>();
    static_for<PD>([&](auto K) { constexpr int ks = 8 * q + K; xf[ks] = lds128(ab + ks * 2048); });
    SB();
    static_for<8>([&](auto K) {
      constexpr int kk = K, ks = 8 * q + kk;
      if constexpr (kk + PD < 8) xf[ks + PD] = lds128(ab + (ks + PD) * 2048);
      if constexpr (ks + PD < 32) wf[ks + PD] = trfrag(wb + (ks + PD) * 2048, wb + (ks + PD) * 2048 + 1024);
      SB();
      acc = mfma32(wf[ks], xf[ks], acc);
      SB();
    });
    PIN(acc);
  });
  return acc;
}
template <int EXTRA> DI void small_helper_bars() {
  wait_bar<6 + EXTRA>(); wait_bar<4 + EXTRA>(); wait_bar<2 + EXTRA>(); wait_bar<0 + EXTRA>();
}

DI void feature_map(const f32x16& p, u32x4 (&f)[4]) {
  float mx = p[0], mn = p[0];
#pragma unroll
  for (int i = 1; i < 16; ++i) { mx = __builtin_fmaxf(mx, p[i]); mn = __builtin_fminf(mn, p[i]); }
  const float a = -mx * LOG2E, bb = mn * LOG2E;
  float ep[16], em[16], sp = 0.f, sm = 0.f;
#pragma unroll
  for (int i = 0; i < 16; ++i) {
    ep[i] = __builtin_amdgcn_exp2f(__builtin_fmaf(p[i], LOG2E, a));
    em[i] = __builtin_amdgcn_exp2f(__builtin_fmaf(p[i], -LOG2E, bb));
    sp += ep[i]; sm += em[i];
  }
  const float rp = __builtin_amdgcn_rcpf(sp), rm = __builtin_amdgcn_rcpf(sm);
#pragma unroll
  for (int s = 0; s < 2; ++s) {
    f[s] = u32x4{pk2(ep[8 * s] * rp, ep[8 * s + 1] * rp), pk2(ep[8 * s + 2] * rp, ep[8 * s + 3] * rp),
                 pk2(ep[8 * s + 4] * rp, ep[8 * s + 5] * rp), pk2(ep[8 * s + 6] * rp, ep[8 * s + 7] * rp)};
    f[2 + s] = u32x4{pk2(em[8 * s] * rm, em[8 * s + 1] * rm), pk2(em[8 * s + 2] * rm, em[8 * s + 3] * rm),
                     pk2(em[8 * s + 4] * rm, em[8 * s + 5] * rm), pk2(em[8 * s + 6] * rm, em[8 * s + 7] * rm)};
  }
}
DI void load_R32(const Ctx& c, int lane, u32x4 (&R)[32], u32 soff) {
  const u32 sb = opq_s(soff);
#pragma unroll
  for (int ks = 0; ks < 32; ++ks) R[ks] = ld128(c, lane * 16 + (ks & 3) * 1024, sb + (ks >> 2) * 4096);
  __builtin_amdgcn_sched_barrier(0);
}

__global__ void __launch_bounds__(512, 2) mega(Params p) {
  Ctx c;
  c.w = __builtin_amdgcn_readfirstlane(threadIdx.x >> 6);
  c.b = blockIdx.x;
  { const int X = c.b & 7, i = c.b >> 3; c.g = X * 4 + (i >> 3); c.j = i & 7; }
  c.ws = (GAS unsigned char*)p.ws;
  c.rs = __builtin_amdgcn_make_buffer_rsrc(p.ws, 0, WS_END, 0x00020000);
  c.srd = mk_srd(p.ws, WS_END);
  c.tag = *(const GAS u64*)(c.ws + WS_EPOCH) + 1;
  const int w = c.w, g = c.g, j = c.j;
  const int G2 = g >> 1, hp = j + 8 * (g & 1);
  STAMP(0);

  f32x16 xres = {};
  u32x4 R[32];
  const int myT = 2 * g + ((w >> 1) & 1), myct = 2 * j + (w & 1);

  {
    const int lane = fresh_lane(), tok = lane & 31, h = lane >> 5;
    const int gq = lane >> 4, q4 = (lane & 15) >> 2, pp = lane & 3;
    u32x2 xin[4];
    if (w < 4) {
      const GAS unsigned short* xp = (const GAS unsigned short*)p.x + (size_t)(32 * myT + tok) * 512 + 32 * myct + 4 * h;
#pragma unroll
      for (int q = 0; q < 4; ++q) xin[q] = *(const GAS u32x2*)(xp + 8 * q);
    }
    const int u = c.b * 7 + w;
    const bool is3 = u < 896;
    const int uu = is3 ? u : u - 896;
    const int cp = is3 ? uu >> 5 : uu / 112, pks = is3 ? uu & 31 : uu % 112;
    if (w < 7) {
      const u32 rowb = is3 ? 3584u : 1024u;
      const u32x4 srd = mk_srd(is3 ? p.w3 : p.w4, 512u * 1792u * 2u);
      const u32 voff = (lane >> 3) * rowb + (lane & 7) * 16 + cp * 128;
      dma_pl<0, 0>(srd, voff, 16 * pks * rowb, L_PT + w * 2048);
      dma_pl<0, 1024>(srd, voff, (16 * pks + 8) * rowb, L_PT + w * 2048);
    }
    if (w >= 4) dma_w1(c, p.wqkv);
    if (w < 4) {
#pragma unroll
      for (int q = 0; q < 4; ++q) {
        xres[4 * q] = bflo(xin[q].x); xres[4 * q + 1] = bfhi(xin[q].x);
        xres[4 * q + 2] = bflo(xin[q].y); xres[4 * q + 3] = bfhi(xin[q].y);
      }
      publish_tile(c, lane, xres, WS_L0 + O_X + myT * 32768 + myct * 2048);
      publish_stats(c, lane, xres, WS_L0 + O_XST + myT * 4096 + myct * 256);
      wait_vm<0>();
    }
    bar_lds();
    set_flag(c, FL_X + g, j);
    if (w == 7) {
      const int r = lane & 31, hr = (r >> 2) & 1, f = (r & 3) + 4 * (r >> 3);
      const GAS unsigned short* wp = (const GAS unsigned short*)p.wphi + (2 * hp + h) * 256 + f;
#pragma unroll
      for (int s = 0; s < 2; ++s) {
        u32x4 fr;
#pragma unroll
        for (int d = 0; d < 4; ++d) {
          const u32 lo = wp[(8 * s + 2 * d) * 16], hi = wp[(8 * s + 2 * d + 1) * 16];
          fr[d] = hr == h ? (lo | (hi << 16)) : 0u;
        }
        sts128(L_WPHI + s * 1024 + lane * 16, fr);
      }
    }
    if (w < 7) {
      if (w >= 4) wait_vm<24>();
      const u32 tb = L_PT + w * 2048 + (4 * (gq >> 1) + q4) * 128 + 32 * (gq & 1) + 8 * pp;
      const u32 dst = (is3 ? WS_W3T + (2 * cp) * 32768 + pks * 1024 : WS_W4T + (2 * cp) * (112 * 1024) + pks * 1024);
      const u32 ctstride = is3 ? 32768u : 112u * 1024u;
#pragma unroll
      for (int cc = 0; cc < 2; ++cc) st128(c, trfrag(tb + cc * 64, tb + cc * 64 + 1024), lane * 16, dst + cc * ctstride);
    }
  }
  STAMP(1);

  for (int L = 0; L < 2; ++L) {
    const u32 LB = WS_L0 + L * SZ_LAYER;
    u32x4 phiq[4];
    if (w < 4) {
      const int lane = fresh_lane(), tok = lane & 31, h = lane >> 5;
      const int gq = lane >> 4, q4 = (lane & 15) >> 2, pp = lane & 3;
      float mean, rstd;
      {
        if (L == 0) wait_vm<0>();
        const int T = 4 * G2 + w;
        poll(c, FL_X + L * 32 + (T >> 1), lane & 7, lane < 8);
        Stat8 st;
        load_stats(c, lane, st, LB + O_XST + T * 4096);
        load_R32(c, lane, R, LB + O_X + T * 32768);
        float var;
        finish_stats(st, mean, var);
        rstd = __builtin_amdgcn_rsqf(var + LN_EPS);
        SB();
        asm volatile("s_barrier" ::: "memory");
      }
      if (L == 0) set_flag(c, FL_PT + (c.b >> 4), c.b & 15);
      STAMP(2 + L * 24);
      const u32 colq = 16 * (pp & 1) + 8 * (gq & 1) + 4 * (pp >> 1);
      const u32 wb = L_W + (4 * (gq >> 1) + q4) * 64 + colq * 2;
      f32x16 aq = {}, ak = {}, av = {};
      {
        const u32 wb2 = opq_v(wb + 65536);
        u32x4 fk[32], fv[32], fq[32];
        auto rd = [&](auto K) {
          constexpr int ks = K;
          fk[ks] = trfrag(wb + 32768 + ks * 1024, wb + 32768 + ks * 1024 + 512);
          fv[ks] = trfrag(wb2 + ks * 1024, wb2 + ks * 1024 + 512);
          fq[ks] = trfrag(wb + ks * 1024, wb + ks * 1024 + 512);
        };
        static_for<2>(rd);
        SB();
        static_for<32>([&](auto K) {
          constexpr int ks = K;
          ak = mfma32(fk[ks], R[ks], ak);
          SB();
          if constexpr (ks + 2 < 32) rd(std::integral_constant<int, ks + 2>{});
          SB();
          av = mfma32(fv[ks], R[ks], av);
          aq = mfma32(fq[ks], R[ks], aq);
          SB();
        });
        PIN(ak); PIN(av); PIN(aq);
      }
      bar_lds();
      STAMP(3 + L * 24);
      const float mr = -mean * rstd;
      const int row = 32 * w + tok;
      {
        const f32x16 cs = load_cs(L_CS1 + 128, h);
#pragma unroll
        for (int i = 0; i < 16; ++i) ak[i] = __builtin_fmaf(ak[i], rstd, mr * cs[i]);
        u32x4 k0, k1; pack2(ak, k0, k1);
        f32x16 pk = {};
        pk = mfma32(lds128(L_WPHI + lane * 16), k0, pk);
        pk = mfma32(lds128(L_WPHI + 1024 + lane * 16), k1, pk);
        u32x4 f[4]; feature_map(pk, f);
#pragma unroll
        for (int i = 0; i < 4; ++i) sts128(L_PK + row * 128 + (((4 * h + i) ^ (row & 7)) * 16), f[i]);
      }
      {
        const f32x16 cs = load_cs(L_CS1 + 256, h);
#pragma unroll
        for (int i = 0; i < 16; ++i) av[i] = __builtin_fmaf(av[i], rstd, mr * cs[i]);
        u32x4 v0, v1; pack2(av, v0, v1);
        sts128(L_V + row * 64 + (((2 * h) ^ ((row >> 1) & 3)) * 16), v0);
        sts128(L_V + row * 64 + (((2 * h + 1) ^ ((row >> 1) & 3)) * 16), v1);
      }
      bar_lds();
      {
        const int hh = w >> 1, sg = w & 1;
        f32x4 kv = {}, z = {};
        const u32x4 ones = {ONES, ONES, ONES, ONES};
        u32x4 fa[4], fb[4];
#pragma unroll
        for (int ks = 0; ks < 4; ++ks) {
          const int r0 = 32 * ks + 8 * gq + q4, r1 = r0 + 4;
          const u32 a0 = L_PK + r0 * 128 + (((4 * hh + 2 * sg + (pp >> 1)) ^ (r0 & 7)) * 16) + 8 * (pp & 1);
          const u32 a1 = L_PK + r1 * 128 + (((4 * hh + 2 * sg + (pp >> 1)) ^ (r1 & 7)) * 16) + 8 * (pp & 1);
          const u32 b0 = L_V + r0 * 64 + (((2 * hh + (pp >> 1)) ^ ((r0 >> 1) & 3)) * 16) + 8 * (pp & 1);
          const u32 b1 = L_V + r1 * 64 + (((2 * hh + (pp >> 1)) ^ ((r1 >> 1) & 3)) * 16) + 8 * (pp & 1);
          fa[ks] = trfrag(a0, a1); fb[ks] = trfrag(b0, b1);
        }
        SB();
#pragma unroll
        for (int ks = 0; ks < 4; ++ks) { kv = mfma16(fa[ks], fb[ks], kv); z = mfma16(fa[ks], ones, z); }
        const u32 rec = LB + O_KVP + (hp * 16 + G2) * 4352;
        st128(c, __builtin_bit_cast(u32x4, kv), lane * 16, rec + w * 1024);
        if ((lane & 15) == 0) st128(c, __builtin_bit_cast(u32x4, z), gq * 16, rec + 4096 + w * 64);
        wait_vm<0>();
      }
      bar_lds();
      set_flag(c, FL_KV + L * 16 + hp, G2);
      STAMP(4 + L * 24);
      {
        const f32x16 cs = load_cs(L_CS1, h);
#pragma unroll
        for (int i = 0; i < 16; ++i) aq[i] = __builtin_fmaf(aq[i], rstd, mr * cs[i]);
        u32x4 q0, q1; pack2(aq, q0, q1);
        f32x16 pq = {};
        pq = mfma32(lds128(L_WPHI + lane * 16), q0, pq);
        pq = mfma32(lds128(L_WPHI + 1024 + lane * 16), q1, pq);
        feature_map(pq, phiq);
      }
    } else {
      const int lane = fresh_lane();
      wait_bar<0>();
      if (L == 0 && w < 7) {
        const int gq = lane >> 4, q4 = (lane & 15) >> 2, pp = lane & 3;
        const u32 colq = 16 * (pp & 1) + 8 * (gq & 1) + 4 * (pp >> 1);
        const u32 wbs = L_W + (4 * (gq >> 1) + q4) * 64 + colq * 2 + (w - 4) * 32768;
        f32x16 a = {};
        const u32x4 ones = {ONES, ONES, ONES, ONES};
#pragma unroll 1
        for (int k4 = 0; k4 < 8; ++k4) {
          u32x4 f[4];
#pragma unroll
          for (int e = 0; e < 4; ++e) f[e] = trfrag(wbs + k4 * 4096 + e * 1024, wbs + k4 * 4096 + e * 1024 + 512);
          SB();
#pragma unroll
          for (int e = 0; e < 4; ++e) a = mfma32(f[e], ones, a);
        }
        store_cs(L_CS1 + (w - 4) * 128, a, lane);
      }
      bar_lds();
      dma_w_small(c, p.wo);
      bar_lds();
      bar_lds();
#pragma unroll
      for (int i = 0; i < 4; ++i) asm volatile("" : "=v"(phiq[i]));
    }
    if (w < 4) {
      const int lane = fresh_lane(), tok = lane & 31, h = lane >> 5, gq = lane >> 4;
      bar_lds();
      STAMP(5 + L * 24);
      {
        const u32 sb = opq_s(LB + O_KVP + hp * 16 * 4352);
        u32x4 v[16];
#pragma unroll
        for (int r = 0; r < 16; ++r) v[r] = ld128(c, w * 1024 + lane * 16, sb + r * 4352);
        SB();
        f32x4 s = __builtin_bit_cast(f32x4, v[0]);
#pragma unroll
        for (int r = 1; r < 16; ++r) s += __builtin_bit_cast(f32x4, v[r]);
        const u32x2 o = {pk2(s[0], s[1]), pk2(s[2], s[3])};
        *(LAS u32x2*)(L_KVT + ((w >> 1) * 16 + (lane & 15)) * 64 + ((w & 1) * 16 + 4 * gq) * 2) = o;
      }
      bar_lds();
      f32x16 num = {}, den = {};
      const bool on = (tok >> 4) == h;
      u32x4 a[4], zz[4];
#pragma unroll
      for (int s = 0; s < 4; ++s) {
        a[s] = lds128(L_KVT + (h * 16 + (tok & 15)) * 64 + s * 16);
        zz[s] = lds128(L_ZT + h * 64 + s * 16);
      }
      SB();
#pragma unroll
      for (int s = 0; s < 4; ++s) {
        if (!on) { a[s] = u32x4{0, 0, 0, 0}; zz[s] = u32x4{0, 0, 0, 0}; }
        num = mfma32(a[s], phiq[s], num);
        den = mfma32(zz[s], phiq[s], den);
      }
      const float r0 = __builtin_amdgcn_rcpf(den[0] + 1e-6f), r1 = __builtin_amdgcn_rcpf(den[8] + 1e-6f);
#pragma unroll
      for (int i = 0; i < 8; ++i) { num[i] *= r0; num[8 + i] *= r1; }
      publish_tile(c, lane, num, LB + O_O + (4 * G2 + w) * 32768 + hp * 2048);
      wait_vm<0>();
      bar_lds();
      set_flag(c, FL_O + L * 16 + G2, hp);
      STAMP(6 + L * 24);
    } else {
      const int lane = fresh_lane();
      if (w == 7) poll(c, FL_KV + L * 16 + hp, lane & 15, lane < 16);
      bar_lds();
      if (w == 4 && lane < 16) {
        const u32 sb = opq_s(LB + O_KVP + hp * 16 * 4352 + 4096);
        u32x4 v[16];
#pragma unroll
        for (int r = 0; r < 16; ++r) v[r] = ld128(c, lane * 16, sb + r * 4352);
        SB();
        f32x4 s = __builtin_bit_cast(f32x4, v[0]);
#pragma unroll
        for (int r = 1; r < 16; ++r) s += __builtin_bit_cast(f32x4, v[r]);
        const u32x2 o = {pk2(s[0], s[1]), pk2(s[2], s[3])};
        *(LAS u32x2*)(L_ZT + ((lane >> 3) & 1) * 64 + (((lane >> 2) & 1) * 16 + 4 * (lane & 3)) * 2) = o;
      }
      bar_lds();
      bar_lds();
    }
    if (w < 4) {
      const int lane = fresh_lane();
      bar_lds();
      STAMP(7 + L * 24);
      small_issue<false>(c, lane, LB + O_O);
      const f32x16 acc = small_compute(c, lane);
      bar_lds();
      STAMP(8 + L * 24);
      xres += acc;
      publish_tile(c, lane, xres, LB + O_XA + myT * 32768 + myct * 2048);
      publish_stats(c, lane, xres, LB + O_XAST + myT * 4096 + myct * 256);
      wait_vm<0>();
      bar_lds();
      set_flag(c, FL_XA + L * 32 + g, j);
      STAMP(9 + L * 24);
    } else {
      const int lane = fresh_lane();
      if (w == 7) poll(c, FL_O + L * 16 + G2, lane & 15, lane < 16);
      wait_bar<0>();
      small_issue<false>(c, lane, LB + O_O);
      small_helper_bars<0>();
      bar_lds();
      dma_w_small(c, p.w1);
      bar_lds();
    }
    float ln_m, ln_r;
    if (w < 4) {
      const int lane = fresh_lane(), tok = lane & 31, h = lane >> 5;
      bar_lds();
      STAMP(10 + L * 24);
      small_issue<false>(c, lane, LB + O_XA);
      const f32x16 acc = small_compute(c, lane);
      bar_lds();
      STAMP(11 + L * 24);
      const float mean = ldsf(L_STM + (w >> 1) * 128 + tok * 4), var = ldsf(L_STR + (w >> 1) * 128 + tok * 4);
      const float r1 = __builtin_amdgcn_rsqf(var + LN_EPS);
      const float r2 = __builtin_amdgcn_rsqf(var * r1 * r1 + LN_EPS);
      ln_m = mean; ln_r = r1;
      const float s = r1 * r2, ms = -mean * s;
      const f32x16 cs = load_cs(L_CS3 + (w & 1) * 128, h);
      f32x16 hv;
#pragma unroll
      for (int i = 0; i < 16; ++i) hv[i] = gelu(__builtin_fmaf(acc[i], s, ms * cs[i]));
      publish_tile(c, lane, hv, LB + O_H + myT * 32768 + myct * 2048);
      wait_vm<0>();
      bar_lds();
      set_flag(c, FL_H + L * 32 + g, j);
      STAMP(12 + L * 24);
    } else {
      const int lane = fresh_lane(), tok = lane & 31;
      if (w == 7) poll(c, FL_XA + L * 32 + g, lane & 7, lane < 8);
      wait_bar<0>();
      small_issue<false>(c, lane, LB + O_XA);
      if (w >= 6) {
        Stat8 st;
        load_stats(c, lane, st, LB + O_XAST + (2 * g + (w - 6)) * 4096);
        SB();
        small_helper_bars<8>();
        float mean, var;
        finish_stats(st, mean, var);
        if (lane < 32) { stsf(L_STM + (w - 6) * 128 + tok * 4, mean); stsf(L_STR + (w - 6) * 128 + tok * 4, var); }
      } else if (L == 0) {
        const u32 wb = small_wb(lane, w & 1);
        const u32x4 ones = {ONES, ONES, ONES, ONES};
        f32x16 a = {};
        static_for<4>([&](auto Q) {
          constexpr int q = Q;
          wait_bar<6 - 2 * q>();
          u32x4 f[8];
          static_for<8>([&](auto K) { constexpr int ks = 8 * q + K; f[K] = trfrag(wb + ks * 2048, wb + ks * 2048 + 1024); });
          SB();
          static_for<8>([&](auto K) { a = mfma32(f[K], ones, a); });
        });
        store_cs(L_CS3 + (w & 1) * 128, a, lane);
      } else {
        small_helper_bars<0>();
      }
      bar_lds();
      dma_w_small(c, p.w2);
      bar_lds();
      ln_m = 0.f; ln_r = 0.f;
    }
    if (w < 4) {
      const int lane = fresh_lane();
      bar_lds();
      STAMP(13 + L * 24);
      small_issue<true>(c, lane, LB + O_H);
      const f32x16 acc = small_compute(c, lane);
      bar_lds();
      STAMP(14 + L * 24);
      f32x16 x1;
      const float mr = -ln_m * ln_r;
#pragma unroll
      for (int i = 0; i < 16; ++i) x1[i] = __builtin_fmaf(xres[i], ln_r, mr) + acc[i];
      publish_tile(c, lane, x1, LB + O_X1 + myT * 32768 + myct * 2048);
      publish_stats(c, lane, x1, LB + O_X1ST + myT * 4096 + myct * 256);
      xres += x1;
      wait_vm<0>();
      bar_lds();
      set_flag(c, FL_X1 + L * 32 + g, j);
    } else {
      const int lane = fresh_lane();
      if (w == 7) {
        const int gg = g + (lane >> 3) - 1;
        poll(c, FL_H + L * 32 + gg, lane & 7, lane < 24 && gg >= 0 && gg < 32);
      }
      if (L == 0 && w == 6) {
        for (int r = 0; r < 4; ++r) poll(c, FL_PT + r * 4 + (lane >> 4), lane & 15, true);
      }
      wait_bar<0>();
      small_issue<true>(c, lane, LB + O_H);
      small_helper_bars<0>();
      bar_lds();
      bar_lds();
    }
    {
      const int lane = fresh_lane();
      if (w < 7) load_R32(c, lane, R, WS_W3T + (7 * j + w) * 32768);
      else undef_R(R);
      STAMP(15 + L * 24);
    }
    if (w < 7) {
      const int lane = fresh_lane(), tok = lane & 31, h = lane >> 5;
      if (L == 0) {
        f32x16 a = {};
        const u32x4 ones = {ONES, ONES, ONES, ONES};
#pragma unroll
        for (int ks = 0; ks < 32; ++ks) a = mfma32(R[ks], ones, a);
        store_cs(L_CS5 + w * 128, a, lane);
      }
      cwait_vm0();
      bar_lds();
      STAMP(16 + L * 24);
      {
        const u32 sb = opq_s(LB + O_X1 + 2 * g * 32768 + w * 1024), lb = opq_s(L_A + w * 1024);
        static_for<8>([&](auto Q) { constexpr int q = Q; dma_sc1<(q >> 2) * 32768 + (q & 3) * 8192, q * 8192>(c.srd, lane * 16, sb, lb); });
      }
      const u32 ab = opq_v(L_A + lane * 16);
      f32x16 a0 = {}, a1 = {};
      static_for<4>([&](auto Q) {
        constexpr int q = Q;
        wait_bar<(q == 3 ? 0 : 7 - q)>();
        u32x4 xf[8];
        static_for<4>([&](auto K) { constexpr int kk = K; xf[kk] = lds128(ab + (8 * q + kk) * 1024); });
        SB();
        static_for<8>([&](auto K) {
          constexpr int kk = K;
          if constexpr (kk + 4 < 8) xf[kk + 4] = lds128(ab + (8 * q + kk + 4) * 1024);
          SB();
          a0 = mfma32(R[8 * q + kk], xf[kk], a0);
          SB();
        });
        PIN(a0);
      });
      bar_lds();
      STAMP(17 + L * 24);
      const f32x16 cs = load_cs(L_CS5 + w * 128, h);
      const u32 ob = LB + O_H2 + (2 * g) * (112 * 1024) + (7 * j + w) * 2048;
      {
        const float mean = ldsf(L_STM + tok * 4), r = ldsf(L_STR + tok * 4), ms = -mean * r;
#pragma unroll
        for (int i = 0; i < 16; ++i) a0[i] = gelu(__builtin_fmaf(a0[i], r, ms * cs[i]));
        publish_tile(c, lane, a0, ob);
      }
      {
        u32x4 xf[32];
        static_for<4>([&](auto K) { constexpr int ks = K; xf[ks] = lds128(ab + (32 + ks) * 1024); });
        SB();
        static_for<32>([&](auto K) {
          constexpr int ks = K;
          if constexpr (ks + 4 < 32) xf[ks + 4] = lds128(ab + (32 + ks + 4) * 1024);
          SB();
          a1 = mfma32(R[ks], xf[ks], a1);
          SB();
        });
        PIN(a1);
      }
      {
        const float mean = ldsf(L_STM + 128 + tok * 4), r = ldsf(L_STR + 128 + tok * 4), ms = -mean * r;
#pragma unroll
        for (int i = 0; i < 16; ++i) a1[i] = gelu(__builtin_fmaf(a1[i], r, ms * cs[i]));
        publish_tile(c, lane, a1, ob + 112 * 1024);
      }
      wait_vm<0>();
      bar_lds();
      set_flag(c, FL_H2 + L * 32 + g, j);
    } else {
      const int lane = fresh_lane(), tok = lane & 31;
      poll(c, FL_X1 + L * 32 + g, lane & 7, lane < 8);
      bar_lds();
      {
        const u32 sb = opq_s(LB + O_X1 + 2 * g * 32768 + w * 1024), lb = opq_s(L_A + w * 1024);
        static_for<8>([&](auto Q) { constexpr int q = Q; dma_sc1<(q >> 2) * 32768 + (q & 3) * 8192, q * 8192>(c.srd, lane * 16, sb, lb); });
      }
      Stat8 st, st2;
      load_stats(c, lane, st, LB + O_X1ST + (2 * g) * 4096);
      load_stats(c, lane, st2, LB + O_X1ST + (2 * g + 1) * 4096);
      SB();
      wait_bar<7 + 16>(); wait_bar<6 + 16>(); wait_bar<5 + 16>(); wait_bar<0>();
      float mean, var;
      finish_stats(st, mean, var);
      if (lane < 32) { stsf(L_STM + tok * 4, mean); stsf(L_STR + tok * 4, __builtin_amdgcn_rsqf(var + LN_EPS)); }
      finish_stats(st2, mean, var);
      if (lane < 32) { stsf(L_STM + 128 + tok * 4, mean); stsf(L_STR + 128 + tok * 4, __builtin_amdgcn_rsqf(var + LN_EPS)); }
      bar_lds();
      bar_lds();
    }
    {
      const int lane = fresh_lane();
      const u32 sb = opq_s(WS_W4T + (2 * j + (w & 1)) * (112 * 1024) + 2 * (w >> 1) * 1024);
#pragma unroll
      for (int q = 0; q < 14; ++q)
#pragma unroll
        for (int e = 0; e < 2; ++e) R[2 * q + e] = ld128(c, lane * 16 + e * 1024, sb + q * 8192);
#pragma unroll
      for (int i = 28; i < 32; ++i) asm volatile("" : "=v"(R[i]));
      SB();
      STAMP(18 + L * 24);
    }
    {
      const int lane = fresh_lane(), tok = lane & 31, h = lane >> 5;
      if (w == 7) poll(c, FL_H2 + L * 32 + g, lane & 7, lane < 8);
      cwait_vm0();
      bar_lds();
      STAMP(19 + L * 24);
      const u32 sb = opq_s(LB + O_H2 + (2 * g) * (112 * 1024) + w * 1024), lb = opq_s(L_A + w * 2048);
      auto issue = [&](auto Q) {
        constexpr int q = Q;
        dma_sc1<q * 8192, (q & 3) * 16384>(c.srd, lane * 16, sb, lb);
        dma_sc1<112 * 1024 + q * 8192, (q & 3) * 16384 + 1024>(c.srd, lane * 16, sb, lb);
      };
      static_for<4>(issue);
      f32x16 a0 = {}, a1 = {};
      const u32 ab = opq_v(L_A + lane * 16 + (w >> 1) * 4096);
      u32x4 xf[14][4];
      auto rd = [&](auto Q) {
        constexpr int q = Q;
        static_for<4>([&](auto E) { constexpr int e = E; xf[q][e] = lds128(ab + (q & 3) * 16384 + (e >> 1) * 2048 + (e & 1) * 1024); });
      };
      wait_bar<6>();
      rd(std::integral_constant<int, 0>{});
      static_for<14>([&](auto Q) {
        constexpr int q = Q;
        if constexpr (q < 13) wait_bar<(q + 3 < 14 ? 4 : q + 2 < 14 ? 2 : 0)>();
        if constexpr (q + 4 < 14) issue(std::integral_constant<int, q + 4>{});
        if constexpr (q + 1 < 14) rd(std::integral_constant<int, q + 1>{});
        SB();
        a0 = mfma32(R[2 * q], xf[q][0], a0);
        a1 = mfma32(R[2 * q], xf[q][1], a1);
        a0 = mfma32(R[2 * q + 1], xf[q][2], a0);
        a1 = mfma32(R[2 * q + 1], xf[q][3], a1);
        SB();
      });
      PIN(a0); PIN(a1);
      bar_lds();
      STAMP(20 + L * 24);
      if (L == 0 && w >= 4) dma_w1(c, p.wqkv);
      {
        const int cc = w & 1, kq = w >> 1;
        if (kq != 0) {
          const u32 sl = L_RED + ((cc * 2 + 0) * 3 + (kq - 1)) * 4096 + lane * 16;
#pragma unroll
          for (int q = 0; q < 4; ++q) sts128f(sl + q * 1024, f32x4{a0[4 * q], a0[4 * q + 1], a0[4 * q + 2], a0[4 * q + 3]});
        }
        if (kq != 1) {
          const u32 sl = L_RED + ((cc * 2 + 1) * 3 + (kq > 1 ? kq - 1 : kq)) * 4096 + lane * 16;
#pragma unroll
          for (int q = 0; q < 4; ++q) sts128f(sl + q * 1024, f32x4{a1[4 * q], a1[4 * q + 1], a1[4 * q + 2], a1[4 * q + 3]});
        }
      }
      bar_lds();
      if (w < 4) {
        const int cc = w & 1, t = w >> 1;
        f32x16 s = t == 0 ? a0 : a1;
#pragma unroll
        for (int r = 0; r < 3; ++r)
#pragma unroll
          for (int q = 0; q < 4; ++q) {
            const f32x4 v = lds128f(L_RED + ((cc * 2 + t) * 3 + r) * 4096 + q * 1024 + lane * 16);
            s[4 * q] += v[0]; s[4 * q + 1] += v[1]; s[4 * q + 2] += v[2]; s[4 * q + 3] += v[3];
          }
        xres += s;
        if (L == 0) {
          publish_tile(c, lane, xres, WS_L0 + SZ_LAYER + O_X + myT * 32768 + myct * 2048);
          publish_stats(c, lane, xres, WS_L0 + SZ_LAYER + O_XST + myT * 4096 + myct * 256);
          wait_vm<0>();
        } else {
          GAS unsigned short* op = (GAS unsigned short*)p.out + (size_t)(32 * myT + tok) * 512 + 32 * myct + 4 * h;
#pragma unroll
          for (int q = 0; q < 4; ++q)
            *(GAS u32x2*)(op + 8 * q) = u32x2{pk2(xres[4 * q], xres[4 * q + 1]), pk2(xres[4 * q + 2], xres[4 * q + 3])};
        }
      }
      if (L == 0) {
        bar_lds();
        set_flag(c, FL_X + 32 + g, j);
      }
      STAMP(21 + L * 24);
    }
  }
  if (c.b == 0 && c.w == 0 && fresh_lane() == 0) *(GAS u64*)(c.ws + WS_EPOCH) = c.tag;
}

}

extern "C" void kernel_launch(void* const* d_in, const int* in_sizes, int n_in, void* d_out, int out_size, void* d_ws,
                              size_t ws_size, hipStream_t stream) {
  Params p;
  p.x = (const unsigned short*)d_in[0];
  p.wqkv = (const unsigned short*)d_in[1];
  p.wphi = (const unsigned short*)d_in[2];
  p.wo = (const unsigned short*)d_in[3];
  p.w1 = (const unsigned short*)d_in[5];
  p.w2 = (const unsigned short*)d_in[6];
  p.w3 = (const unsigned short*)d_in[7];
  p.w4 = (const unsigned short*)d_in[8];
  p.out = (unsigned short*)d_out;
  p.ws = (unsigned char*)d_ws;
  (void)hipFuncSetAttribute((const void*)mega, hipFuncAttributeMaxDynamicSharedMemorySize, LDS_BYTES);
  hipLaunchKernelGGL(mega, dim3(256), dim3(512), LDS_BYTES, stream, p);
}
```

```cpp
#include <hip/hip_runtime.h>
#include <hip/hip_bf16.h>
#include <type_traits>

#define GAS __attribute__((address_space(1)))
#define LAS __attribute__((address_space(3)))
#define DI __device__ __forceinline__
#define RLX_AGENT __ATOMIC_RELAXED, __HIP_MEMORY_SCOPE_AGENT
#pragma clang diagnostic ignored "-Wint-to-pointer-cast"
#define SB() __builtin_amdgcn_sched_barrier(0)
#define PIN(x) asm volatile("" : "+v"(x))

typedef unsigned u32;
typedef unsigned long long u64;
typedef u32 u32x4 __attribute__((ext_vector_type(4)));
typedef u32 u32x2 __attribute__((ext_vector_type(2)));
typedef float f32x2 __attribute__((ext_vector_type(2)));
typedef float f32x4 __attribute__((ext_vector_type(4)));
typedef float f32x16 __attribute__((ext_vector_type(16)));
typedef __bf16 bf16x2 __attribute__((ext_vector_type(2)));
typedef __bf16 bf16x8 __attribute__((ext_vector_type(8)));
typedef short s16x4 __attribute__((ext_vector_type(4)));

#define WARM 1
#define WPOS 1
#define STAG 1
#define PA 0
#define PB 8
#define PC 8
#define PD_ 20
#define PE 24
#define WOPOS 0
#define W1A 8
#define NA 8
#define ABL6 0
#define WARMH2 1
#define WARMX 1
#define FAKEW 0
#define XA 10
#define NOW 0
#define NLF 10
#define PUB0 14
namespace {

constexpr u32 WS_EPOCH = 0, WS_TMO = 128, WS_FLAGS = 4096;
constexpr u32 FL_X = 0, FL_KV = 64, FL_O = 96, FL_XA = 128, FL_H = 192, FL_X1 = 256, FL_H2 = 320, FL_PT = 384, FL_H2B = 400;
constexpr u32 SZ_ACT = 2048u * 512u * 2u, SZ_ST = 64u * 4096u, SZ_KVP = 256u * 2176u, SZ_H2 = 2048u * 1792u * 2u;
constexpr u32 O_X = 0, O_XST = O_X + SZ_ACT, O_KVP = O_XST + SZ_ST, O_O = O_KVP + SZ_KVP, O_XA = O_O + SZ_ACT,
              O_XAST = O_XA + SZ_ACT, O_H = O_XAST + SZ_ST, O_X1 = O_H + SZ_ACT, O_X1ST = O_X1 + SZ_ACT,
              O_H2 = O_X1ST + SZ_ST, SZ_LAYER = O_H2 + SZ_H2;
constexpr u32 WS_L0 = 65536, WS_W3T = WS_L0 + 2 * SZ_LAYER, SZ_WT = 512u * 1792u * 2u, WS_W4T = WS_W3T + SZ_WT,
              WS_DBG = WS_W4T + SZ_WT, WS_END = WS_DBG + 256u * 128u * 8u + 256u * 32u * 8u;
static_assert(SZ_LAYER % 128 == 0, "align");

constexpr u32 L_CS1 = 0;
constexpr u32 L_CS3 = 512;
constexpr u32 L_CS5 = 1024;
constexpr u32 L_STM = 2048;
constexpr u32 L_STR = 2304;
constexpr u32 L_WPHI = 2560;
constexpr u32 L_KVT = 4608;
constexpr u32 L_ZT = 6656;
constexpr u32 L_CNT = 6912;
constexpr u32 L_DUMMY = 7168;
constexpr u32 L_W = 8192;
constexpr u32 L_A = 98304;
constexpr u32 L_PK = 106496;
constexpr u32 L_V = 122880;
constexpr u32 L_PT = 131072;
constexpr u32 L_RED = 114688;
constexpr u32 LDS_BYTES = 163840;

constexpr float LN_EPS = 1e-5f;
constexpr float LOG2E = 1.4426950408889634f;
constexpr u32 SPIN_CAP = 1u << 17;
constexpr u32 ONES = 0x3f803f80u;

struct Params {
  const unsigned short *x, *wqkv, *wphi, *wo, *w1, *w2, *w3, *w4;
  unsigned short* out;
  unsigned char* ws;
};

template <int N, class F> DI void static_for(F&& f) {
  if constexpr (N > 0) { static_for<N - 1>(f); f(std::integral_constant<int, N - 1>{}); }
}
DI u32 opq_s(u32 x) { asm volatile("" : "+s"(x)); return x; }
DI u32 opq_v(u32 x) { asm volatile("" : "+v"(x)); return x; }
DI int fresh_lane() {
  int l;
  asm volatile("v_mbcnt_lo_u32_b32 %0, -1, 0\n\tv_mbcnt_hi_u32_b32 %0, -1, %0" : "=v"(l));
  return l;
}
DI u32 pk2(float a, float b) {
  f32x2 v = {a, b};
  return __builtin_bit_cast(u32, __builtin_convertvector(v, bf16x2));
}
DI float as_f(u32 x) { return __builtin_bit_cast(float, x); }
DI u32 as_u(float x) { return __builtin_bit_cast(u32, x); }
DI float bflo(u32 u) { return __builtin_bit_cast(float, u << 16); }
DI float bfhi(u32 u) { return __builtin_bit_cast(float, u & 0xffff0000u); }
DI f32x16 mfma32(u32x4 a, u32x4 b, f32x16 c) {
  return __builtin_amdgcn_mfma_f32_32x32x16_bf16(__builtin_bit_cast(bf16x8, a), __builtin_bit_cast(bf16x8, b), c, 0, 0, 0);
}
DI f32x4 mfma16(u32x4 a, u32x4 b, f32x4 c) {
  return __builtin_amdgcn_mfma_f32_16x16x32_bf16(__builtin_bit_cast(bf16x8, a), __builtin_bit_cast(bf16x8, b), c, 0, 0, 0);
}
DI u32x4 lds128(u32 a) { return *(LAS u32x4*)a; }
DI f32x4 lds128f(u32 a) { return *(LAS f32x4*)a; }
DI float ldsf(u32 a) { return *(LAS float*)a; }
DI void stsf(u32 a, float v) { *(LAS float*)a = v; }
DI void sts128(u32 a, u32x4 v) { *(LAS u32x4*)a = v; }
DI void sts128f(u32 a, f32x4 v) { *(LAS f32x4*)a = v; }
DI u32x4 trfrag(u32 a0, u32 a1) {
  s16x4 lo = __builtin_amdgcn_ds_read_tr16_b64_v4i16((LAS s16x4*)a0);
  s16x4 hi = __builtin_amdgcn_ds_read_tr16_b64_v4i16((LAS s16x4*)a1);
  return __builtin_bit_cast(u32x4, __builtin_shufflevector(lo, hi, 0, 1, 2, 3, 4, 5, 6, 7));
}
DI u32x4 mk_srd(const void* p, u32 bytes) {
  const u64 a = (u64)p;
  return u32x4{(u32)a, (u32)(a >> 32) & 0xffffu, bytes, 0x00020000u};
}
template <u32 SC, u32 LC> DI void dma_sc1(u32x4 srd, u32 voff, u32 sb, u32 lb) {
  u32 t;
  asm volatile("s_add_u32 m0, %1, %2\n\ts_add_u32 %0, %3, %4\n\tbuffer_load_dwordx4 %5, %6, %0 offen sc1 lds"
               : "=&s"(t) : "s"(lb), "i"(LC), "s"(sb), "i"(SC), "v"(voff), "s"(srd) : "memory", "scc");
}
template <u32 SC, u32 LC> DI void dma_pl(u32x4 srd, u32 voff, u32 sb, u32 lb) {
  u32 t;
  asm volatile("s_add_u32 m0, %1, %2\n\ts_add_u32 %0, %3, %4\n\tbuffer_load_dwordx4 %5, %6, %0 offen lds"
               : "=&s"(t) : "s"(lb), "i"(LC), "s"(sb), "i"(SC), "v"(voff), "s"(srd) : "memory", "scc");
}
template <int N> DI void wait_bar() { asm volatile("s_waitcnt vmcnt(%0) lgkmcnt(0)\n\ts_barrier" ::"i"(N) : "memory"); }
template <int N> DI void wait_vm() { asm volatile("s_waitcnt vmcnt(%0)" ::"i"(N) : "memory"); }
DI void cwait_vm0() { __builtin_amdgcn_s_waitcnt(0x0F70); }
DI void bar_lds() { asm volatile("s_waitcnt lgkmcnt(0)\n\ts_barrier" ::: "memory"); }
DI void undef_R(u32x4 (&R)[32]) {
#pragma unroll
  for (int i = 0; i < 32; ++i) asm volatile("" : "=v"(R[i]));
}
DI float xhalf_sum(float v) {
  auto r = __builtin_amdgcn_permlane32_swap(as_u(v), as_u(v), false, false);
  return as_f(r[0]) + as_f(r[1]);
}
DI f32x2 pr(const f32x16& v, int i) { return f32x2{v[2 * i], v[2 * i + 1]}; }
DI void setpr(f32x16& v, int i, f32x2 x) { v[2 * i] = x[0]; v[2 * i + 1] = x[1]; }
DI f32x2 bc2(float a) { return f32x2{a, a}; }
DI f32x2 fma2(f32x2 a, f32x2 b, f32x2 c) { return __builtin_elementwise_fma(a, b, c); }
DI void ln_fold(f32x16& v, float s, float ms, const f32x16& cs) {
#pragma unroll
  for (int i = 0; i < 8; ++i) setpr(v, i, fma2(pr(v, i), bc2(s), pr(cs, i) * bc2(ms)));
}
DI void gelu16(f32x16& x) {
  const float c1 = -2.3022081986f, c2 = -0.1029432397f;
  f32x16 t;
#pragma unroll
  for (int i = 0; i < 8; ++i) { const f32x2 a = pr(x, i); setpr(t, i, fma2(a * a, bc2(c2), bc2(c1))); }
  SB();
#pragma unroll
  for (int i = 0; i < 8; ++i) setpr(t, i, pr(t, i) * pr(x, i));
  SB();
#pragma unroll
  for (int i = 0; i < 16; ++i) t[i] = __builtin_amdgcn_exp2f(t[i]);
  SB();
#pragma unroll
  for (int i = 0; i < 8; ++i) setpr(t, i, pr(t, i) + bc2(1.0f));
  SB();
#pragma unroll
  for (int i = 0; i < 16; ++i) t[i] = __builtin_amdgcn_rcpf(t[i]);
  SB();
#pragma unroll
  for (int i = 0; i < 8; ++i) setpr(x, i, pr(x, i) * pr(t, i));
  SB();
}
DI float sum16(const f32x16& v) {
  f32x2 a = pr(v, 0) + pr(v, 1), b = pr(v, 2) + pr(v, 3), c = pr(v, 4) + pr(v, 5), d = pr(v, 6) + pr(v, 7);
  a += b; c += d; a += c;
  return a[0] + a[1];
}
DI float sumsq16(const f32x16& v) {
  f32x2 a = pr(v, 0) * pr(v, 0), b = pr(v, 1) * pr(v, 1), c = pr(v, 2) * pr(v, 2), d = pr(v, 3) * pr(v, 3);
  a = fma2(pr(v, 4), pr(v, 4), a); b = fma2(pr(v, 5), pr(v, 5), b); c = fma2(pr(v, 6), pr(v, 6), c); d = fma2(pr(v, 7), pr(v, 7), d);
  a += b; c += d; a += c;
  return a[0] + a[1];
}

struct Ctx {
  __amdgpu_buffer_rsrc_t rs;
  u32x4 srd;
  GAS unsigned char* ws;
  u64 tag;
  int w, g, j, b;
};

DI void poll(const Ctx& c, u32 line_of_lane, u32 slot, bool active) {
  const GAS u64* p = (const GAS u64*)(c.ws + WS_FLAGS + line_of_lane * 128u + slot * 8u);
  GAS u32* tmo = (GAS u32*)(c.ws + WS_TMO);
  for (u32 spins = 1;; ++spins) {
    u64 v = c.tag;
    if (active) v = __hip_atomic_load(p, RLX_AGENT);
    if (__all(v == c.tag)) break;
    if ((spins & 63u) == 0u) {
      if (__builtin_amdgcn_readfirstlane(__hip_atomic_load(tmo, RLX_AGENT)) == (u32)c.tag) break;
      if (spins > SPIN_CAP) { __hip_atomic_store(tmo, (u32)c.tag, RLX_AGENT); break; }
    }
  }
  __builtin_amdgcn_fence(__ATOMIC_ACQUIRE, "wavefront");
}
DI void set_flag(const Ctx& c, u32 line, u32 slot) {
  if (c.w == 0) {
    if (fresh_lane() == 0) __hip_atomic_store((GAS u64*)(c.ws + WS_FLAGS + line * 128u + slot * 8u), c.tag, RLX_AGENT);
  }
}
DI void publish(const Ctx& c, int cnt, u32 nwaves, u32 line, u32 slot) {
  asm volatile("s_waitcnt vmcnt(0)" ::: "memory");
  if (fresh_lane() == 0) {
    const u32 old = __hip_atomic_fetch_add((LAS u32*)(L_CNT + cnt * 4), 1u, __ATOMIC_RELAXED, __HIP_MEMORY_SCOPE_WORKGROUP);
    if (old == nwaves - 1) {
      u64 tg = c.tag;
      asm volatile("" : "+s"(tg));
      __hip_atomic_store((GAS u64*)(c.ws + WS_FLAGS + line * 128u + slot * 8u), tg, RLX_AGENT);
    }
  }
}
DI void wait_cnt(int cnt, u32 n) {
  for (u32 spins = 0; spins < (1u << 24); ++spins) {
    const u32 v = __builtin_amdgcn_readfirstlane(__hip_atomic_load((LAS u32*)(L_CNT + cnt * 4), __ATOMIC_RELAXED, __HIP_MEMORY_SCOPE_WORKGROUP));
    if (v >= n) break;
    __builtin_amdgcn_s_sleep(1);
  }
  asm volatile("" ::: "memory");
}
DI void st128(const Ctx& c, u32x4 v, u32 voff, u32 soff) { __builtin_amdgcn_raw_buffer_store_b128(v, c.rs, voff, soff, 16); }
DI u32x4 ld128(const Ctx& c, u32 voff, u32 soff) { return __builtin_amdgcn_raw_buffer_load_b128(c.rs, voff, soff, 16); }

template <int N, u32 STRIDE> DI void warm(const Ctx& c, int lane, u32 soff) {
  if (WARM) {
    const u32 sb = opq_s(soff);
    static_for<N>([&](auto I) { constexpr int i = I; dma_sc1<(i & 1) * 1024 + (i >> 1) * STRIDE, 0>(c.srd, lane * 16, sb, L_DUMMY); });
  }
}
DI void warm_stats(const Ctx& c, int lane, u32 soff) {
  if (WARM) { if (lane < 16) dma_sc1<0, 0>(c.srd, lane * 16, soff, L_DUMMY); }
}
DI void pack2(const f32x16& v, u32x4& f0, u32x4& f1) {
  f0 = u32x4{pk2(v[0], v[1]), pk2(v[2], v[3]), pk2(v[4], v[5]), pk2(v[6], v[7])};
  f1 = u32x4{pk2(v[8], v[9]), pk2(v[10], v[11]), pk2(v[12], v[13]), pk2(v[14], v[15])};
}
DI void publish_tile(const Ctx& c, int lane, const f32x16& v, u32 soff) {
  u32x4 f0, f1;
  pack2(v, f0, f1);
  st128(c, f0, lane * 16, soff);
  st128(c, f1, lane * 16 + 1024, soff);
}
DI void publish_stats(const Ctx& c, int lane, const f32x16& v, u32 soff) {
  float s = sum16(v), q = sumsq16(v);
  s = xhalf_sum(s); q = xhalf_sum(q);
  const u32x2 o = {as_u(s), as_u(q)};
  if (lane < 32) __builtin_amdgcn_raw_buffer_store_b64(o, c.rs, lane * 8, soff, 16);
}
DI u32x2 sum_bf16x4(const u32x2 (&v)[16]) {
  f32x2 a = {0.f, 0.f}, b = {0.f, 0.f};
#pragma unroll
  for (int r = 0; r < 16; ++r) {
    const u32 x = v[r].x, y = v[r].y;
    a += f32x2{bflo(x), bfhi(x)};
    b += f32x2{bflo(y), bfhi(y)};
  }
  return u32x2{pk2(a[0], a[1]), pk2(b[0], b[1])};
}
struct Stat8 { u32x2 v[8]; };
DI void load_stats(const Ctx& c, int lane, Stat8& st, u32 soff) {
  const u32 vo = (lane >> 5) * 2048 + (lane & 31) * 8;
#pragma unroll
  for (int i = 0; i < 8; ++i) st.v[i] = __builtin_amdgcn_raw_buffer_load_b64(c.rs, vo + i * 256, soff, 16);
}
DI void load_stats_asm(const Ctx& c, int lane, Stat8& st, u32 soff) {
  const u32 vo = (lane >> 5) * 2048 + (lane & 31) * 8;
#define LS1(i) asm volatile("buffer_load_dwordx2 %0, %1, %2, %3 offen offset:%4 sc1" : "=v"(st.v[i]) : "v"(vo), "s"(c.srd), "s"(soff), "i"(i * 256) : "memory")
  LS1(0); LS1(1); LS1(2); LS1(3); LS1(4); LS1(5); LS1(6); LS1(7);
#undef LS1
}
DI void landed_stats(Stat8& st) {
  asm volatile("" : "+v"(st.v[0]), "+v"(st.v[1]), "+v"(st.v[2]), "+v"(st.v[3]), "+v"(st.v[4]), "+v"(st.v[5]), "+v"(st.v[6]), "+v"(st.v[7]));
}
DI void finish_stats(const Stat8& st, float& mean, float& var) {
  f32x2 a[8];
#pragma unroll
  for (int i = 0; i < 8; ++i) { const u32 x = st.v[i].x, y = st.v[i].y; a[i] = f32x2{as_f(x), as_f(y)}; }
  a[0] += a[1]; a[2] += a[3]; a[4] += a[5]; a[6] += a[7]; a[0] += a[2]; a[4] += a[6]; a[0] += a[4];
  const float s = xhalf_sum(a[0][0]), q = xhalf_sum(a[0][1]);
  mean = s * (1.0f / 512.0f);
  var = __builtin_fmaxf(__builtin_fmaf(-mean, mean, q * (1.0f / 512.0f)), 0.f);
}
DI f32x16 load_cs(u32 base, int h) {
  f32x16 r;
#pragma unroll
  for (int q = 0; q < 4; ++q) {
    const f32x4 t = lds128f(base + h * 16 + 32 * q);
    r[4 * q] = t[0]; r[4 * q + 1] = t[1]; r[4 * q + 2] = t[2]; r[4 * q + 3] = t[3];
  }
  return r;
}
DI void store_cs(u32 base, const f32x16& v, int lane) {
  if ((lane & 31) == 0) {
#pragma unroll
    for (int q = 0; q < 4; ++q) sts128f(base + (lane >> 5) * 16 + 32 * q, f32x4{v[4 * q], v[4 * q + 1], v[4 * q + 2], v[4 * q + 3]});
  }
}

#define STAMP(i) do { } while (0)
#define XSTAMP(i) do { } while (0)

template <int LO, int HI> DI void dma_w_small(const Ctx& c, const unsigned short* wp) {
  if (NOW) return;
  const int lane = fresh_lane();
  const u32x4 srd = mk_srd(wp, 512u * 512u * 2u);
  const int r = lane >> 3;
  const u32 voff = r * 1024 + (((lane & 7) ^ ((r & 2) << 1)) * 16) + 128 * c.j;
  const u32 sb = opq_s((c.w - 4) * 16 * 8192), lb = opq_s(L_W + (c.w - 4) * 16 * 1024);
  static_for<HI - LO>([&](auto I) { constexpr int i = LO + I; dma_pl<FAKEW ? (i & 7) * 8192 : i * 8192, i * 1024>(srd, voff, FAKEW ? 0u : sb, lb); });
}
template <int LO, int HI> DI void dma_w1(const Ctx& c, const unsigned short* wp) {
  if (NOW) return;
  const int lane = fresh_lane();
  const u32x4 srd = mk_srd(wp, 512u * 1536u * 2u);
  const u32 voff = (lane >> 2) * 3072 + (lane & 3) * 16 + 64 * (c.j + 8 * (c.g & 1));
  const u32 sb = opq_s((c.w - 4) * 8 * 49152), lb = opq_s(L_W + (c.w - 4) * 8 * 1024);
  static_for<HI - LO>([&](auto I) {
    constexpr int i = LO + I, sec = i / 8, n = i % 8;
    dma_pl<FAKEW ? (n & 3) * 49152 + sec * 1024 : n * 49152 + sec * 1024, sec * 32768 + n * 1024>(srd, voff, FAKEW ? 0u : sb, lb);
  });
}

template <bool SHIFT, int Q>
DI void small_issue(const Ctx& c, int lane, u32 sb, u32 lb) {
  constexpr int q = Q, d = q == 0 ? -1 : q == 1 ? 1 : q == 2 ? -2 : 2;
  static_for<2>([&](auto T) {
    constexpr int t = T;
    if constexpr (SHIFT) {
      const int tau = 64 * c.g + 32 * t + (lane & 31) + d;
      const u32 vo = (tau < 0 || tau >= 2048) ? 0x80000000u : (u32)((tau >> 5) * 32768 + ((tau & 31) + (lane & 32)) * 16);
      dma_sc1<q * 8192, q * 16384 + t * 1024>(c.srd, vo, sb, lb);
      dma_sc1<q * 8192 + 1024, q * 16384 + 2048 + t * 1024>(c.srd, vo, sb, lb);
    } else {
      dma_sc1<t * 32768 + q * 8192, q * 16384 + t * 1024>(c.srd, lane * 16, sb, lb);
      dma_sc1<t * 32768 + q * 8192 + 1024, q * 16384 + 2048 + t * 1024>(c.srd, lane * 16, sb, lb);
    }
  });
}
template <bool SHIFT, int EX, class E, class F>
DI void small_feed(const Ctx& c, int lane, u32 abase, E&& extra, F&& between) {
  const int hw = c.w - 4;
  const u32 sb = opq_s(abase + (SHIFT ? 0 : 2 * c.g * 32768) + hw * 2048), lb = opq_s(L_A + hw * 4096);
  small_issue<SHIFT, 0>(c, lane, sb, lb);
  small_issue<SHIFT, 1>(c, lane, sb, lb);
  extra();
  wait_bar<4 + EX>();
  small_issue<SHIFT, 2>(c, lane, sb, lb);
  between(std::integral_constant<int, 0>{});
  wait_bar<4 + EX>();
  small_issue<SHIFT, 3>(c, lane, sb, lb);
  between(std::integral_constant<int, 1>{});
  wait_bar<4>();
  between(std::integral_constant<int, 2>{});
  wait_bar<0>();
  between(std::integral_constant<int, 3>{});
}
DI u32 small_wb(int lane, int cc) {
  const int gq = lane >> 4, q4 = (lane & 15) >> 2, p = lane & 3;
  return L_W + (4 * (gq >> 1) + q4) * 128 + (((4 * cc + 2 * (gq & 1) + (p >> 1)) ^ ((q4 & 2) << 1)) * 16) + 8 * (p & 1);
}
DI f32x16 small_compute(const Ctx& c, int lane) {
  const int w = c.w;
  const u32 wb = opq_v(small_wb(lane, w & 1));
  const u32 ab = opq_v(L_A + (w >> 1) * 1024 + lane * 16);
  f32x16 acc = {};
  u32x4 wf[32], xf[32];
  constexpr int PD = 3;
  static_for<4>([&](auto Q) {
    constexpr int q = Q;
    bar_lds();
    if constexpr (q == 0) static_for<PD>([&](auto K) { constexpr int ks = K; wf[ks] = trfrag(wb + ks * 2048, wb + ks * 2048 + 1024); });
    static_for<PD>([&](auto K) { constexpr int ks = 8 * q + K; xf[ks] = lds128(ab + ks * 2048); });
    SB();
    static_for<8>([&](auto K) {
      constexpr int kk = K, ks = 8 * q + kk;
      if constexpr (kk + PD < 8) xf[ks + PD] = lds128(ab + (ks + PD) * 2048);
      if constexpr (ks + PD < 32) wf[ks + PD] = trfrag(wb + (ks + PD) * 2048, wb + (ks + PD) * 2048 + 1024);
      SB();
      acc = mfma32(wf[ks], xf[ks], acc);
      SB();
    });
    PIN(acc);
  });
  return acc;
}
struct Epi { f32x16 t; u32x4 f0, f1; };
template <int S> DI void epi_step(f32x16& x, Epi& e, float r, float ms, const f32x4 (&cs)[4]) {
  const float c1 = -2.3022081986f, c2 = -0.1029432397f;
  if constexpr (S < 4) {
#pragma unroll
    for (int i = 0; i < 4; ++i) x[4 * S + i] = __builtin_fmaf(x[4 * S + i], r, ms * cs[S][i]);
  } else if constexpr (S < 8) {
#pragma unroll
    for (int i = 4 * (S - 4); i < 4 * (S - 4) + 4; ++i) e.t[i] = __builtin_fmaf(x[i] * x[i], c2, c1) * x[i];
  } else if constexpr (S < 14) {
#pragma unroll
    for (int i = 3 * (S - 8); i < 3 * (S - 8) + 3 && i < 16; ++i) e.t[i] = __builtin_amdgcn_exp2f(e.t[i]);
  } else if constexpr (S < 16) {
#pragma unroll
    for (int i = 8 * (S - 14); i < 8 * (S - 14) + 8; ++i) e.t[i] += 1.0f;
  } else if constexpr (S < 22) {
#pragma unroll
    for (int i = 3 * (S - 16); i < 3 * (S - 16) + 3 && i < 16; ++i) e.t[i] = __builtin_amdgcn_rcpf(e.t[i]);
  } else if constexpr (S < 25) {
#pragma unroll
    for (int i = 6 * (S - 22); i < 6 * (S - 22) + 6 && i < 16; ++i) x[i] *= e.t[i];
  } else if constexpr (S == 25) {
    e.f0 = u32x4{pk2(x[0], x[1]), pk2(x[2], x[3]), pk2(x[4], x[5]), pk2(x[6], x[7])};
  } else if constexpr (S == 26) {
    e.f1 = u32x4{pk2(x[8], x[9]), pk2(x[10], x[11]), pk2(x[12], x[13]), pk2(x[14], x[15])};
  }
}
DI float max3(float a, float b, float c) { return __builtin_fmaxf(__builtin_fmaxf(a, b), c); }
DI float min3(float a, float b, float c) { return __builtin_fminf(__builtin_fminf(a, b), c); }
DI void feature_map(const f32x16& p, u32x4 (&f)[4]) {
  const float mx = max3(max3(max3(p[0], p[1], p[2]), max3(p[3], p[4], p[5]), max3(p[6], p[7], p[8])),
                        max3(max3(p[9], p[10], p[11]), max3(p[12], p[13], p[14]), p[15]), p[15]);
  const float mn = min3(min3(min3(p[0], p[1], p[2]), min3(p[3], p[4], p[5]), min3(p[6], p[7], p[8])),
                        min3(min3(p[9], p[10], p[11]), min3(p[12], p[13], p[14]), p[15]), p[15]);
  const float a = -mx * LOG2E, bb = mn * LOG2E;
  f32x16 ep, em;
  SB();
#pragma unroll
  for (int i = 0; i < 8; ++i) { setpr(ep, i, fma2(pr(p, i), bc2(LOG2E), bc2(a))); setpr(em, i, fma2(pr(p, i), bc2(-LOG2E), bc2(bb))); }
  SB();
#pragma unroll
  for (int i = 0; i < 16; ++i) { ep[i] = __builtin_amdgcn_exp2f(ep[i]); em[i] = __builtin_amdgcn_exp2f(em[i]); }
  SB();
  const float rp = __builtin_amdgcn_rcpf(sum16(ep)), rm = __builtin_amdgcn_rcpf(sum16(em));
  SB();
#pragma unroll
  for (int i = 0; i < 8; ++i) { setpr(ep, i, pr(ep, i) * bc2(rp)); setpr(em, i, pr(em, i) * bc2(rm)); }
  SB();
  pack2(ep, f[0], f[1]);
  pack2(em, f[2], f[3]);
}
DI void load_R32(const Ctx& c, int lane, u32x4 (&R)[32], u32 soff) {
  const u32 sb = opq_s(soff);
#pragma unroll
  for (int ks = 0; ks < 32; ++ks) R[ks] = ld128(c, lane * 16 + (ks & 3) * 1024, sb + (ks >> 2) * 4096);
  __builtin_amdgcn_sched_barrier(0);
}

template <int LO, int HI> DI void load_R(const Ctx& c, int lane, u32x4 (&R)[32], u32 soff) {
  const u32 sb = opq_s(soff);
#pragma unroll
  for (int ks = LO; ks < HI; ++ks) R[ks] = ld128(c, lane * 16 + (ks & 3) * 1024, sb + (ks >> 2) * 4096);
  SB();
}
template <int LO, int HI> DI void load_W3(const Ctx& c, int lane, u32x4 (&R)[32], u32 soff) {
  if (NOW) {
#pragma unroll
    for (int i = LO; i < HI; ++i) asm volatile("" : "=v"(R[i]));
  } else if (FAKEW) {
#pragma unroll
    for (int i = LO; i < HI; ++i) R[i] = ld128(c, lane * 16 + (i & 3) * 1024, opq_s(WS_W3T + c.j * 32768 + ((i >> 2) & 7) * 4096));
    SB();
  } else load_R<LO, HI>(c, lane, R, soff);
}
template <int LO, int HI> DI void undef_part(u32x4 (&R)[32]) {
#pragma unroll
  for (int i = LO; i < HI; ++i) asm volatile("" : "=v"(R[i]));
}

__global__ void __launch_bounds__(512, 2) mega(Params p) {
  Ctx c;
  c.w = __builtin_amdgcn_readfirstlane(threadIdx.x >> 6);
  c.b = blockIdx.x;
  { const int X = c.b & 7, i = c.b >> 3; c.g = X * 4 + (i >> 3); c.j = i & 7; }
  c.ws = (GAS unsigned char*)p.ws;
  c.rs = __builtin_amdgcn_make_buffer_rsrc(p.ws, 0, WS_END, 0x00020000);
  c.srd = mk_srd(p.ws, WS_END);
  c.tag = *(const GAS u64*)(c.ws + WS_EPOCH) + 1;
  const int w = c.w, g = c.g, j = c.j;
  const int G2 = g >> 1, hp = j + 8 * (g & 1);
  STAMP(0);

  f32x16 xres = {};
  u32x4 R[32];
  const int myT = 2 * g + ((w >> 1) & 1), myct = 2 * j + (w & 1);

  const u32 w3base = WS_W3T + (7 * j + w) * 32768;
  {
    const int lane = fresh_lane(), tok = lane & 31, h = lane >> 5;
    const int gq = lane >> 4, q4 = (lane & 15) >> 2, pp = lane & 3;
    if (w == 7 && lane < 32) *(LAS u32*)(L_CNT + lane * 4) = 0u;
    u32x2 xin[4];
    if (w < 4) {
      const GAS unsigned short* xp = (const GAS unsigned short*)p.x + (size_t)(32 * myT + tok) * 512 + 32 * myct + 4 * h;
#pragma unroll
      for (int q = 0; q < 4; ++q) xin[q] = *(const GAS u32x2*)(xp + 8 * q);
    }
    bar_lds();
    if (w < 4) {
#pragma unroll
      for (int q = 0; q < 4; ++q) {
        xres[4 * q] = bflo(xin[q].x); xres[4 * q + 1] = bfhi(xin[q].x);
        xres[4 * q + 2] = bflo(xin[q].y); xres[4 * q + 3] = bfhi(xin[q].y);
      }
      publish_tile(c, lane, xres, WS_L0 + O_X + myT * 32768 + myct * 2048);
      publish_stats(c, lane, xres, WS_L0 + O_XST + myT * 4096 + myct * 256);
      publish(c, 0, 4, FL_X + g, j);
      warm<2, 0>(c, lane, WS_L0 + O_X + myT * 32768 + myct * 2048);
      warm_stats(c, lane, WS_L0 + O_XST + myT * 4096 + myct * 256);
    } else {
      dma_w1<0, 24>(c, p.wqkv);
      const int ne = w == 7 ? 1 : 2;
      for (int ee = 0; ee < ne; ++ee) {
        const int e = (w - 4) * 2 + ee, u = c.b * 7 + e;
        const bool is3 = u < 896;
        const int uu = is3 ? u : u - 896;
        const int cp = is3 ? uu >> 5 : uu / 112, pks = is3 ? uu & 31 : uu % 112;
        const u32 rowb = is3 ? 3584u : 1024u;
        const u32x4 srd = mk_srd(is3 ? p.w3 : p.w4, 512u * 1792u * 2u);
        const u32 voff = (lane >> 3) * rowb + (lane & 7) * 16 + cp * 128;
        dma_pl<0, 0>(srd, voff, 16 * pks * rowb, L_PT + e * 2048);
        dma_pl<0, 1024>(srd, voff, (16 * pks + 8) * rowb, L_PT + e * 2048);
      }
      if (w == 7) {
        const int r = lane & 31, hr = (r >> 2) & 1, f = (r & 3) + 4 * (r >> 3);
        const GAS unsigned short* wp = (const GAS unsigned short*)p.wphi + (2 * hp + h) * 256 + f;
#pragma unroll
        for (int s = 0; s < 2; ++s) {
          u32x4 fr;
#pragma unroll
          for (int d = 0; d < 4; ++d) {
            const u32 lo = wp[(8 * s + 2 * d) * 16], hi = wp[(8 * s + 2 * d + 1) * 16];
            fr[d] = hr == h ? (lo | (hi << 16)) : 0u;
          }
          sts128(L_WPHI + s * 1024 + lane * 16, fr);
        }
      }
      wait_vm<0>();
      for (int ee = 0; ee < ne; ++ee) {
        const int e = (w - 4) * 2 + ee, u = c.b * 7 + e;
        const bool is3 = u < 896;
        const int uu = is3 ? u : u - 896;
        const int cp = is3 ? uu >> 5 : uu / 112, pks = is3 ? uu & 31 : uu % 112;
        const u32 tb = L_PT + e * 2048 + (4 * (gq >> 1) + q4) * 128 + 32 * (gq & 1) + 8 * pp;
        const u32 dst = (is3 ? WS_W3T + (2 * cp) * 32768 + pks * 1024 : WS_W4T + (2 * cp) * (112 * 1024) + pks * 1024);
        const u32 ctstride = is3 ? 32768u : 112u * 1024u;
#pragma unroll
        for (int cc = 0; cc < 2; ++cc) st128(c, trfrag(tb + cc * 64, tb + cc * 64 + 1024), lane * 16, dst + cc * ctstride);
      }
    }
  }
  STAMP(1);

  for (int L = 0; L < 2; ++L) {
    const u32 LB = WS_L0 + L * SZ_LAYER;
    const int cb = 2 + L * 8;
    u32x4 phiq[4];
    if (w < 4) {
      const int lane = fresh_lane(), tok = lane & 31, h = lane >> 5;
      const int gq = lane >> 4, q4 = (lane & 15) >> 2, pp = lane & 3;
      float mean = 0.f, rstd = 0.f;
      u32 xb;
      Stat8 st;
      {
        const int T = 4 * G2 + w;
        poll(c, FL_X + L * 32 + (T >> 1), lane & 7, lane < 8);
        xb = opq_s(LB + O_X + T * 32768);
        load_R<0, 2>(c, lane, R, xb);
        load_stats(c, lane, st, LB + O_XST + T * 4096);
        load_R<2, XA>(c, lane, R, xb);
        asm volatile("s_barrier" ::: "memory");
      }
      STAMP(2 + L * 24);
      const u32 colq = 16 * (pp & 1) + 8 * (gq & 1) + 4 * (pp >> 1);
      const u32 wb = L_W + (4 * (gq >> 1) + q4) * 64 + colq * 2;
      f32x16 aq = {}, ak = {}, av = {};
      {
        const u32 wb2 = opq_v(wb + 65536);
        u32x4 fk[32], fv[32], fq[32];
        auto rd = [&](auto K) {
          constexpr int ks = K;
          fk[ks] = trfrag(wb + 32768 + ks * 1024, wb + 32768 + ks * 1024 + 512);
          fv[ks] = trfrag(wb2 + ks * 1024, wb2 + ks * 1024 + 512);
          fq[ks] = trfrag(wb + ks * 1024, wb + ks * 1024 + 512);
        };
        static_for<2>(rd);
        SB();
        static_for<32>([&](auto K) {
          constexpr int ks = K;
          ak = mfma32(fk[ks], R[ks], ak);
          SB();
          if constexpr (ks + 2 < 32) rd(std::integral_constant<int, ks + 2>{});
          if constexpr (ks + XA < 32) R[ks + XA] = ld128(c, lane * 16 + ((ks + XA) & 3) * 1024, xb + ((ks + XA) >> 2) * 4096);
          SB();
          av = mfma32(fv[ks], R[ks], av);
          aq = mfma32(fq[ks], R[ks], aq);
          SB();
          if constexpr (ks == 2) {
            float var;
            finish_stats(st, mean, var);
            rstd = __builtin_amdgcn_rsqf(var + LN_EPS);
            SB();
          }
        });
        PIN(ak); PIN(av); PIN(aq);
      }
      bar_lds();
      STAMP(3 + L * 24);
      if (L == 0) {
        for (int r = 0; r < 4; ++r) poll(c, FL_PT + r * 4 + (lane >> 4), lane & 15, true);
      }
      load_W3<0, PA>(c, lane, R, w3base);
      const float mr = -mean * rstd;
      const int row = 32 * w + tok;
      {
        const f32x16 cs = load_cs(L_CS1 + 128, h);
        ln_fold(ak, rstd, mr, cs);
        u32x4 k0, k1; pack2(ak, k0, k1);
        f32x16 pk = {};
        pk = mfma32(lds128(L_WPHI + lane * 16), k0, pk);
        pk = mfma32(lds128(L_WPHI + 1024 + lane * 16), k1, pk);
        const f32x16 csv = load_cs(L_CS1 + 256, h);
        ln_fold(av, rstd, mr, csv);
        u32x4 v0, v1; pack2(av, v0, v1);
        sts128(L_V + row * 64 + (((2 * h) ^ ((row >> 1) & 3)) * 16), v0);
        sts128(L_V + row * 64 + (((2 * h + 1) ^ ((row >> 1) & 3)) * 16), v1);
        SB();
        u32x4 f[4]; feature_map(pk, f);
        XSTAMP(49);
#pragma unroll
        for (int i = 0; i < 4; ++i) sts128(L_PK + row * 128 + (((4 * h + i) ^ (row & 7)) * 16), f[i]);
      }
      bar_lds();
      XSTAMP(50);
      {
        const int hh = w >> 1, sg = w & 1;
        f32x4 kv = {}, z = {};
        const u32x4 ones = {ONES, ONES, ONES, ONES};
        u32x4 fa[4], fb[4];
#pragma unroll
        for (int ks = 0; ks < 4; ++ks) {
          const int r0 = 32 * ks + 8 * gq + q4, r1 = r0 + 4;
          const u32 a0 = L_PK + r0 * 128 + (((4 * hh + 2 * sg + (pp >> 1)) ^ (r0 & 7)) * 16) + 8 * (pp & 1);
          const u32 a1 = L_PK + r1 * 128 + (((4 * hh + 2 * sg + (pp >> 1)) ^ (r1 & 7)) * 16) + 8 * (pp & 1);
          const u32 b0 = L_V + r0 * 64 + (((2 * hh + (pp >> 1)) ^ ((r0 >> 1) & 3)) * 16) + 8 * (pp & 1);
          const u32 b1 = L_V + r1 * 64 + (((2 * hh + (pp >> 1)) ^ ((r1 >> 1) & 3)) * 16) + 8 * (pp & 1);
          fa[ks] = trfrag(a0, a1); fb[ks] = trfrag(b0, b1);
        }
        SB();
#pragma unroll
        for (int ks = 0; ks < 4; ++ks) { kv = mfma16(fa[ks], fb[ks], kv); z = mfma16(fa[ks], ones, z); }
        const u32 rec = LB + O_KVP + (hp * 16 + G2) * 2176;
        __builtin_amdgcn_raw_buffer_store_b64(u32x2{pk2(kv[0], kv[1]), pk2(kv[2], kv[3])}, c.rs, lane * 8, rec + w * 512, 16);
        if ((lane & 15) == 0) __builtin_amdgcn_raw_buffer_store_b64(u32x2{pk2(z[0], z[1]), pk2(z[2], z[3])}, c.rs, gq * 8, rec + 2048 + w * 32, 16);
        XSTAMP(51);
        publish(c, cb + 0, 4, FL_KV + L * 16 + hp, G2);
        XSTAMP(52);
      }
      load_W3<PA, PB>(c, lane, R, w3base);
      STAMP(4 + L * 24);
      {
        const f32x16 cs = load_cs(L_CS1, h);
        ln_fold(aq, rstd, mr, cs);
        u32x4 q0, q1; pack2(aq, q0, q1);
        f32x16 pq = {};
        pq = mfma32(lds128(L_WPHI + lane * 16), q0, pq);
        pq = mfma32(lds128(L_WPHI + 1024 + lane * 16), q1, pq);
        feature_map(pq, phiq);
      }
    } else {
      const int lane = fresh_lane();
      if (L == 0) asm volatile("s_waitcnt lgkmcnt(0)\n\ts_barrier" ::: "memory");
      else wait_bar<0>();
      if (L == 0) publish(c, 1, 4, FL_PT + (c.b >> 4), c.b & 15);
      if (L == 0 && w < 7) {
        const int gq = lane >> 4, q4 = (lane & 15) >> 2, pp = lane & 3;
        const u32 colq = 16 * (pp & 1) + 8 * (gq & 1) + 4 * (pp >> 1);
        const u32 wbs = L_W + (4 * (gq >> 1) + q4) * 64 + colq * 2 + (w - 4) * 32768;
        f32x16 a = {};
        const u32x4 ones = {ONES, ONES, ONES, ONES};
#pragma unroll 1
        for (int k4 = 0; k4 < 8; ++k4) {
          u32x4 f[4];
#pragma unroll
          for (int e = 0; e < 4; ++e) f[e] = trfrag(wbs + k4 * 4096 + e * 1024, wbs + k4 * 4096 + e * 1024 + 512);
          SB();
#pragma unroll
          for (int e = 0; e < 4; ++e) a = mfma32(f[e], ones, a);
        }
        store_cs(L_CS1 + (w - 4) * 128, a, lane);
      }
      bar_lds();
      if (w < 7) {
        if (L == 0) { for (int r = 0; r < 4; ++r) poll(c, FL_PT + r * 4 + (lane >> 4), lane & 15, true); }
        load_W3<0, PA>(c, lane, R, w3base);
      } else undef_part<0, PA>(R);
      if (WOPOS == 0) dma_w_small<0, 8>(c, p.wo);
      bar_lds();
      if (WOPOS == 0) dma_w_small<8, 16>(c, p.wo);
      wait_cnt(cb + 0, 4);
      if (WOPOS == 1) dma_w_small<0, 16>(c, p.wo);
      if (w < 7) load_W3<PA, PB>(c, lane, R, w3base); else undef_part<PA, PB>(R);
#pragma unroll
      for (int i = 0; i < 4; ++i) asm volatile("" : "=v"(phiq[i]));
    }
    if (w < 4) {
      const int lane = fresh_lane(), tok = lane & 31, h = lane >> 5, gq = lane >> 4;
      bar_lds();
      STAMP(5 + L * 24);
      {
        const u32 sb = opq_s(LB + O_KVP + hp * 16 * 2176);
        u32x2 v[16];
#pragma unroll
        for (int r = 0; r < 16; ++r) v[r] = __builtin_amdgcn_raw_buffer_load_b64(c.rs, w * 512 + lane * 8, sb + r * 2176, 16);
        SB();
        load_W3<PB, PC>(c, lane, R, w3base);
        const u32x2 o = sum_bf16x4(v);
        *(LAS u32x2*)(L_KVT + ((w >> 1) * 16 + (lane & 15)) * 64 + ((w & 1) * 16 + 4 * gq) * 2) = o;
        XSTAMP(58);
      }
      bar_lds();
      XSTAMP(59);
      f32x16 num = {}, den = {};
      const bool on = (tok >> 4) == h;
      u32x4 a[4], zz[4];
#pragma unroll
      for (int s = 0; s < 4; ++s) {
        a[s] = lds128(L_KVT + (h * 16 + (tok & 15)) * 64 + s * 16);
        zz[s] = lds128(L_ZT + h * 64 + s * 16);
      }
      SB();
#pragma unroll
      for (int s = 0; s < 4; ++s) {
        if (!on) { a[s] = u32x4{0, 0, 0, 0}; zz[s] = u32x4{0, 0, 0, 0}; }
        num = mfma32(a[s], phiq[s], num);
        den = mfma32(zz[s], phiq[s], den);
      }
      const float r0 = __builtin_amdgcn_rcpf(den[0] + 1e-6f), r1 = __builtin_amdgcn_rcpf(den[8] + 1e-6f);
#pragma unroll
      for (int i = 0; i < 4; ++i) { setpr(num, i, pr(num, i) * bc2(r0)); setpr(num, 4 + i, pr(num, 4 + i) * bc2(r1)); }
      publish_tile(c, lane, num, LB + O_O + (4 * G2 + w) * 32768 + hp * 2048);
      XSTAMP(60);
      publish(c, cb + 1, 4, FL_O + L * 16 + G2, hp);
      XSTAMP(61);
      warm<2, 0>(c, lane, LB + O_O + (4 * G2 + w) * 32768 + hp * 2048);
      load_W3<PC, PD_>(c, lane, R, w3base);
      STAMP(6 + L * 24);
    } else {
      const int lane = fresh_lane();
      if (w == 7) poll(c, FL_KV + L * 16 + hp, lane & 15, lane < 16);
      bar_lds();
      if (w == 5 || w == 6) load_W3<PB, PC>(c, lane, R, w3base);
      if (w == 4 && lane < 16) {
        const u32 sb = opq_s(LB + O_KVP + hp * 16 * 2176 + 2048);
        u32x2 v[16];
#pragma unroll
        for (int r = 0; r < 16; ++r) v[r] = __builtin_amdgcn_raw_buffer_load_b64(c.rs, lane * 8, sb + r * 2176, 16);
        SB();
        const u32x2 o = sum_bf16x4(v);
        *(LAS u32x2*)(L_ZT + ((lane >> 3) & 1) * 64 + (((lane >> 2) & 1) * 16 + 4 * (lane & 3)) * 2) = o;
      }
      if (w == 4) load_W3<PB, PC>(c, lane, R, w3base);
      if (w == 7) undef_part<PB, PC>(R);
      bar_lds();
      wait_cnt(cb + 1, 4);
      if (w < 7) load_W3<PC, PD_>(c, lane, R, w3base); else undef_part<PC, PD_>(R);
    }
    if (w < 4) {
      const int lane = fresh_lane();
      bar_lds();
      STAMP(7 + L * 24);
      const f32x16 acc = small_compute(c, lane);
      bar_lds();
      STAMP(8 + L * 24);
      xres += acc;
      publish_tile(c, lane, xres, LB + O_XA + myT * 32768 + myct * 2048);
      publish_stats(c, lane, xres, LB + O_XAST + myT * 4096 + myct * 256);
      publish(c, cb + 2, 4, FL_XA + L * 32 + g, j);
      warm<2, 0>(c, lane, LB + O_XA + myT * 32768 + myct * 2048);
      warm_stats(c, lane, LB + O_XAST + myT * 4096 + myct * 256);
      STAMP(9 + L * 24);
    } else {
      const int lane = fresh_lane();
      if (w == 7) poll(c, FL_O + L * 16 + G2, lane & 15, lane < 16);
      bar_lds();
      small_feed<false, 0>(c, lane, LB + O_O, [] {}, [](auto) {});
      bar_lds();
      if (WPOS == 1) wait_cnt(cb + 2, 4);
      dma_w_small<0, 16>(c, p.w1);
    }
    float ln_m, ln_r;
    if (w < 4) {
      const int lane = fresh_lane(), tok = lane & 31, h = lane >> 5;
      bar_lds();
      STAMP(10 + L * 24);
      const f32x16 acc = small_compute(c, lane);
      bar_lds();
      STAMP(11 + L * 24);
      const float mean = ldsf(L_STM + (w >> 1) * 128 + tok * 4), var = ldsf(L_STR + (w >> 1) * 128 + tok * 4);
      const float r1 = __builtin_amdgcn_rsqf(var + LN_EPS);
      const float r2 = __builtin_amdgcn_rsqf(var * r1 * r1 + LN_EPS);
      ln_m = mean; ln_r = r1;
      const float s = r1 * r2, ms = -mean * s;
      const f32x16 cs = load_cs(L_CS3 + (w & 1) * 128, h);
      f32x16 hv = acc;
      ln_fold(hv, s, ms, cs);
      SB();
      gelu16(hv);
      publish_tile(c, lane, hv, LB + O_H + myT * 32768 + myct * 2048);
      publish(c, cb + 3, 4, FL_H + L * 32 + g, j);
      warm<2, 0>(c, lane, LB + O_H + myT * 32768 + myct * 2048);
      load_W3<PD_, PE>(c, lane, R, w3base);
      STAMP(12 + L * 24);
    } else {
      const int lane = fresh_lane(), tok = lane & 31;
      if (w == 7) poll(c, FL_XA + L * 32 + g, lane & 7, lane < 8);
      bar_lds();
      if (w >= 6) {
        Stat8 st;
        const u32 so = opq_s(LB + O_XAST + (2 * g + (w - 6)) * 4096);
        small_feed<false, 8>(c, lane, LB + O_XA, [&] { load_stats_asm(c, lane, st, so); }, [&](auto Q) {
          if constexpr (Q == 2) {
            landed_stats(st);
            float mean, var;
            finish_stats(st, mean, var);
            if (lane < 32) { stsf(L_STM + (w - 6) * 128 + tok * 4, mean); stsf(L_STR + (w - 6) * 128 + tok * 4, var); }
          }
        });
      } else if (L == 0) {
        const u32 wb = small_wb(lane, w & 1);
        const u32x4 ones = {ONES, ONES, ONES, ONES};
        f32x16 a = {};
        small_feed<false, 0>(c, lane, LB + O_XA, [] {}, [&](auto Q) {
          constexpr int q = Q;
          u32x4 f[8];
          static_for<8>([&](auto K) { constexpr int ks = 8 * q + K; f[K] = trfrag(wb + ks * 2048, wb + ks * 2048 + 1024); });
          SB();
          static_for<8>([&](auto K) { a = mfma32(f[K], ones, a); });
        });
        store_cs(L_CS3 + (w & 1) * 128, a, lane);
      } else {
        small_feed<false, 0>(c, lane, LB + O_XA, [] {}, [](auto) {});
      }
      bar_lds();
      if (WPOS == 1) wait_cnt(cb + 3, 4);
      dma_w_small<0, 16>(c, p.w2);
      if (w < 7) load_W3<PD_, PE>(c, lane, R, w3base); else undef_part<PD_, PE>(R);
      ln_m = 0.f; ln_r = 0.f;
    }
    if (w < 4) {
      const int lane = fresh_lane();
      bar_lds();
      STAMP(13 + L * 24);
      const f32x16 acc = small_compute(c, lane);
      bar_lds();
      STAMP(14 + L * 24);
      f32x16 x1;
      const float mr = -ln_m * ln_r;
#pragma unroll
      for (int i = 0; i < 8; ++i) setpr(x1, i, fma2(pr(xres, i), bc2(ln_r), bc2(mr)) + pr(acc, i));
      publish_tile(c, lane, x1, LB + O_X1 + myT * 32768 + myct * 2048);
      publish_stats(c, lane, x1, LB + O_X1ST + myT * 4096 + myct * 256);
      xres += x1;
      publish(c, cb + 4, 4, FL_X1 + L * 32 + g, j);
      warm<2, 0>(c, lane, LB + O_X1 + myT * 32768 + myct * 2048);
      warm_stats(c, lane, LB + O_X1ST + myT * 4096 + myct * 256);
      load_W3<PE, 32>(c, lane, R, w3base);
      STAMP(15 + L * 24);
    } else {
      const int lane = fresh_lane();
      if (w == 7) {
        const int gg = g + (lane >> 3) - 1;
        const int part = lane >> 3;
        poll(c, FL_H + L * 32 + gg, lane & 7, lane < 24 && gg >= 0 && gg < 32 && (part == 1 || ((lane & 2) != 0) == (part == 2)));
      }
      bar_lds();
      small_feed<true, 0>(c, lane, LB + O_H, [] {}, [](auto) {});
      bar_lds();
      if (w < 7) { wait_cnt(cb + 4, 4); load_W3<PE, 32>(c, lane, R, w3base); } else undef_part<PE, 32>(R);
    }
    u32x4 R4[28 - NLF];
    const u32 w4base = WS_W4T + (2 * j + (w & 1)) * (112 * 1024) + 2 * (w >> 1) * 1024;
    auto r4_load = [&](int lane, u32 sb4, auto I) {
      constexpr int i = I, f = NLF + i;
      if (NOW) asm volatile("" : "=v"(R4[i])); else if (FAKEW) R4[i] = ld128(c, lane * 16 + (i & 3) * 1024, opq_s(WS_W4T + c.j * 32768 + (i >> 2) * 4096)); else R4[i] = ld128(c, lane * 16 + (f & 1) * 1024, sb4 + (f >> 1) * 8192);
    };
    if (w < 7) {
      const int lane = fresh_lane(), tok = lane & 31, h = lane >> 5;
      if (L == 0) {
        f32x16 a = {};
        const u32x4 ones = {ONES, ONES, ONES, ONES};
#pragma unroll
        for (int ks = 0; ks < 32; ++ks) a = mfma32(R[ks], ones, a);
        store_cs(L_CS5 + w * 128, a, lane);
      }
      bar_lds();
      STAMP(16 + L * 24);
      const u32 ab = opq_v(L_A + lane * 16);
      f32x16 a0 = {}, a1 = {};
      static_for<4>([&](auto Q) {
        constexpr int q = Q;
        bar_lds();
        u32x4 xf[8];
        static_for<4>([&](auto K) { constexpr int kk = K; xf[kk] = lds128(ab + (8 * q + kk) * 1024); });
        SB();
        static_for<8>([&](auto K) {
          constexpr int kk = K;
          if constexpr (kk + 4 < 8) xf[kk + 4] = lds128(ab + (8 * q + kk + 4) * 1024);
          SB();
          a0 = mfma32(R[8 * q + kk], xf[kk], a0);
          SB();
        });
        PIN(a0);
      });
      bar_lds();
      STAMP(17 + L * 24);
      const u32 ob = LB + O_H2 + (2 * g) * (112 * 1024) + (7 * j + w) * 2048;
      {
        const float mean = ldsf(L_STM + tok * 4), r = ldsf(L_STR + tok * 4), ms = -mean * r;
        const u32 sb4 = opq_s(w4base);
        Epi e;
        f32x4 cs[4];
#pragma unroll
        for (int q = 0; q < 4; ++q) cs[q] = lds128f(L_CS5 + w * 128 + h * 16 + 32 * q);
        u32x4 xf[32];
        static_for<3>([&](auto K) { constexpr int ks = K; xf[ks] = lds128(ab + (32 + ks) * 1024); });
        SB();
        static_for<32>([&](auto K) {
          constexpr int ks = K;
          if constexpr (ks + 3 < 32) xf[ks + 3] = lds128(ab + (32 + ks + 3) * 1024);
          SB();
          a1 = mfma32(R[ks], xf[ks], a1);
          SB();
          if constexpr (ks >= 1 && ks <= 27) { epi_step<ks - 1>(a0, e, r, ms, cs); SB(); }
          if constexpr (ks == 28) { st128(c, e.f0, lane * 16, ob); st128(c, e.f1, lane * 16 + 1024, ob); SB(); }
          if constexpr (ks >= 2 && (ks % 2 == 0) && (ks - 2) / 2 < NA) {
            r4_load(lane, sb4, std::integral_constant<int, (ks - 2) / 2>{});
            SB();
          }
        });
        PIN(a1);
        XSTAMP(54);
      }
      {
        const float mean = ldsf(L_STM + 128 + tok * 4), r = ldsf(L_STR + 128 + tok * 4), ms = -mean * r;
        const f32x16 cs = load_cs(L_CS5 + w * 128, h);
        ln_fold(a1, r, ms, cs);
        SB();
        gelu16(a1);
        publish_tile(c, lane, a1, ob + 112 * 1024);
        XSTAMP(55);
      }
      publish(c, cb + 5, 7, FL_H2 + L * 32 + g, j);
      XSTAMP(56);
      if (WARMH2) warm<4, 112 * 1024>(c, lane, ob);
      {
        const u32 sb4 = opq_s(w4base);
        static_for<28 - NLF - NA>([&](auto I) { r4_load(lane, sb4, std::integral_constant<int, NA + I>{}); });
        SB();
      }
      STAMP(18 + L * 24);
    } else {
      const int lane = fresh_lane(), tok = lane & 31;
      poll(c, FL_X1 + L * 32 + g, lane & 7, lane < 8);
      bar_lds();
      {
        const u32 sb = opq_s(LB + O_X1 + 2 * g * 32768), lb = opq_s(L_A);
        auto chunk = [&](auto Q) {
          constexpr int q = Q;
          static_for<8>([&](auto K) { constexpr int f = 8 * q + K; dma_sc1<f * 1024, f * 1024>(c.srd, lane * 16, sb, lb); });
        };
        chunk(std::integral_constant<int, 0>{});
        chunk(std::integral_constant<int, 1>{});
        wait_bar<8>();
        chunk(std::integral_constant<int, 2>{});
        wait_bar<8>();
        chunk(std::integral_constant<int, 3>{});
        wait_bar<8>();
        static_for<4>([&](auto Q) { chunk(std::integral_constant<int, 4 + Q>{}); });
        Stat8 st, st2;
        load_stats(c, lane, st, LB + O_X1ST + (2 * g) * 4096);
        load_stats(c, lane, st2, LB + O_X1ST + (2 * g + 1) * 4096);
        SB();
        wait_bar<32 + 16>();
        float mean, var;
        finish_stats(st, mean, var);
        if (lane < 32) { stsf(L_STM + tok * 4, mean); stsf(L_STR + tok * 4, __builtin_amdgcn_rsqf(var + LN_EPS)); }
        finish_stats(st2, mean, var);
        if (lane < 32) { stsf(L_STM + 128 + tok * 4, mean); stsf(L_STR + 128 + tok * 4, __builtin_amdgcn_rsqf(var + LN_EPS)); }
      }
      wait_bar<0>();
      {
        static_for<8>([&](auto WW) {
          constexpr int ww = WW;
          const u32 sb = opq_s(WS_W4T + (2 * j + (ww & 1)) * (112 * 1024) + 2 * (ww >> 1) * 1024);
          const u32 lb = opq_s(L_W + ww * NLF * 1024);
          static_for<NLF>([&](auto I) {
            constexpr int f = I;
            if (!NOW) dma_sc1<FAKEW ? f * 1024 : (f >> 1) * 8192 + (f & 1) * 1024, f * 1024>(c.srd, lane * 16, FAKEW ? WS_W4T + c.j * 32768 : sb, lb);
          });
        });
      }
      {
        const u32 sb4 = opq_s(w4base);
        static_for<28 - NLF>([&](auto I) { r4_load(lane, sb4, I); });
        SB();
      }
    }
    {
      const int lane = fresh_lane(), tok = lane & 31, h = lane >> 5;
      if (w == 7) { poll(c, FL_H2 + L * 32 + g, lane & 7, lane < 8); wait_vm<0>(); }
      bar_lds();
      STAMP(19 + L * 24);
      const u32 sb = opq_s(LB + O_H2 + (2 * g) * (112 * 1024) + w * 1024), lb = opq_s(L_A + w * 2048);
      auto issue1 = [&](auto Q, auto T) {
        constexpr int q = Q, t = T;
        dma_sc1<t * 112 * 1024 + q * 8192, (q & 3) * 16384 + t * 1024>(c.srd, lane * 16, sb, lb);
      };
      static_for<4>([&](auto Q) { issue1(Q, std::integral_constant<int, 0>{}); issue1(Q, std::integral_constant<int, 1>{}); });
      f32x16 a0 = {}, a1 = {};
      const u32 ab = opq_v(L_A + lane * 16 + (w >> 1) * 4096);
      const u32 wl = opq_v(L_W + w * NLF * 1024 + lane * 16);
      u32x4 xf[14][4], wf[14][2];
      auto rd = [&](auto Q) {
        constexpr int q = Q;
        static_for<4>([&](auto E) { constexpr int e = E; xf[q][e] = lds128(ab + (q & 3) * 16384 + (e >> 1) * 2048 + (e & 1) * 1024); });
        static_for<2>([&](auto E) { constexpr int e = E; if constexpr (2 * q + e < NLF) wf[q][e] = lds128(wl + (2 * q + e) * 1024); });
      };
      wait_bar<6>();
      XSTAMP(62);
      rd(std::integral_constant<int, 0>{});
      static_for<14>([&](auto Q) {
        constexpr int q = Q;
        if constexpr (q < 13) wait_bar<(q + 3 < 14 ? 4 : q + 2 < 14 ? 2 : 0)>();
        if constexpr (q + 1 < 14) rd(std::integral_constant<int, q + 1>{});
        SB();
        u32x4 w0, w1;
        if constexpr (2 * q < NLF) w0 = wf[q][0]; else w0 = R4[2 * q - NLF];
        if constexpr (2 * q + 1 < NLF) w1 = wf[q][1]; else w1 = R4[2 * q + 1 - NLF];
        a0 = mfma32(w0, xf[q][0], a0);
        SB();
        if constexpr (q + 4 < 14) issue1(std::integral_constant<int, q + 4>{}, std::integral_constant<int, 0>{});
        SB();
        a1 = mfma32(w0, xf[q][1], a1);
        a0 = mfma32(w1, xf[q][2], a0);
        SB();
        if constexpr (q + 4 < 14) issue1(std::integral_constant<int, q + 4>{}, std::integral_constant<int, 1>{});
        SB();
        a1 = mfma32(w1, xf[q][3], a1);
        SB();
      });
      PIN(a0); PIN(a1);
      bar_lds();
      STAMP(20 + L * 24);
      {
        const int cc = w & 1, kq = w >> 1;
        if (kq != 0) {
          const u32 sl = L_RED + ((cc * 2 + 0) * 3 + (kq - 1)) * 4096 + lane * 16;
#pragma unroll
          for (int q = 0; q < 4; ++q) sts128f(sl + q * 1024, f32x4{a0[4 * q], a0[4 * q + 1], a0[4 * q + 2], a0[4 * q + 3]});
        }
        if (kq != 1) {
          const u32 sl = L_RED + ((cc * 2 + 1) * 3 + (kq > 1 ? kq - 1 : kq)) * 4096 + lane * 16;
#pragma unroll
          for (int q = 0; q < 4; ++q) sts128f(sl + q * 1024, f32x4{a1[4 * q], a1[4 * q + 1], a1[4 * q + 2], a1[4 * q + 3]});
        }
      }
      bar_lds();
      if (w < 4) {
        const int cc = w & 1, t = w >> 1;
        f32x16 s = t == 0 ? a0 : a1;
        f32x4 v[3][4];
#pragma unroll
        for (int r = 0; r < 3; ++r)
#pragma unroll
          for (int q = 0; q < 4; ++q) v[r][q] = lds128f(L_RED + ((cc * 2 + t) * 3 + r) * 4096 + q * 1024 + lane * 16);
        SB();
#pragma unroll
        for (int r = 0; r < 3; ++r)
#pragma unroll
          for (int q = 0; q < 4; ++q) { s[4 * q] += v[r][q][0]; s[4 * q + 1] += v[r][q][1]; s[4 * q + 2] += v[r][q][2]; s[4 * q + 3] += v[r][q][3]; }
        xres += s;
        if (L == 0) {
          publish_tile(c, lane, xres, WS_L0 + SZ_LAYER + O_X + myT * 32768 + myct * 2048);
          publish_stats(c, lane, xres, WS_L0 + SZ_LAYER + O_XST + myT * 4096 + myct * 256);
          publish(c, cb + 6, 4, FL_X + 32 + g, j);
          if (WARMX) warm<2, 0>(c, lane, WS_L0 + SZ_LAYER + O_X + myT * 32768 + myct * 2048);
          warm_stats(c, lane, WS_L0 + SZ_LAYER + O_XST + myT * 4096 + myct * 256);
        } else {
          GAS unsigned short* op = (GAS unsigned short*)p.out + (size_t)(32 * myT + tok) * 512 + 32 * myct + 4 * h;
#pragma unroll
          for (int q = 0; q < 4; ++q)
            *(GAS u32x2*)(op + 8 * q) = u32x2{pk2(xres[4 * q], xres[4 * q + 1]), pk2(xres[4 * q + 2], xres[4 * q + 3])};
        }
      } else if (L == 0) {
        dma_w1<0, W1A>(c, p.wqkv);
        wait_cnt(cb + 6, 4);
        dma_w1<W1A, 24>(c, p.wqkv);
      }
      STAMP(21 + L * 24);
    }
  }
  if (c.b == 0 && c.w == 0 && fresh_lane() == 0) *(GAS u64*)(c.ws + WS_EPOCH) = c.tag;
}

}

extern "C" void kernel_launch(void* const* d_in, const int* in_sizes, int n_in, void* d_out, int out_size, void* d_ws,
                              size_t ws_size, hipStream_t stream) {
  Params p;
  p.x = (const unsigned short*)d_in[0];
  p.wqkv = (const unsigned short*)d_in[1];
  p.wphi = (const unsigned short*)d_in[2];
  p.wo = (const unsigned short*)d_in[3];
  p.w1 = (const unsigned short*)d_in[5];
  p.w2 = (const unsigned short*)d_in[6];
  p.w3 = (const unsigned short*)d_in[7];
  p.w4 = (const unsigned short*)d_in[8];
  p.out = (unsigned short*)d_out;
  p.ws = (unsigned char*)d_ws;
  (void)hipFuncSetAttribute((const void*)mega, hipFuncAttributeMaxDynamicSharedMemorySize, LDS_BYTES);
  hipLaunchKernelGGL(mega, dim3(256), dim3(512), LDS_BYTES, stream, p);
}
```

```cpp
#include <hip/hip_runtime.h>
#include <hip/hip_bf16.h>
#include <type_traits>

#define GAS __attribute__((address_space(1)))
#define LAS __attribute__((address_space(3)))
#define DI __device__ __forceinline__
#define RLX_AGENT __ATOMIC_RELAXED, __HIP_MEMORY_SCOPE_AGENT
#pragma clang diagnostic ignored "-Wint-to-pointer-cast"
#define SB() __builtin_amdgcn_sched_barrier(0)
#define PIN(x) asm volatile("" : "+v"(x))

typedef unsigned u32;
typedef unsigned long long u64;
typedef u32 u32x4 __attribute__((ext_vector_type(4)));
typedef u32 u32x2 __attribute__((ext_vector_type(2)));
typedef float f32x2 __attribute__((ext_vector_type(2)));
typedef float f32x4 __attribute__((ext_vector_type(4)));
typedef float f32x16 __attribute__((ext_vector_type(16)));
typedef __bf16 bf16x2 __attribute__((ext_vector_type(2)));
typedef __bf16 bf16x8 __attribute__((ext_vector_type(8)));
typedef short s16x4 __attribute__((ext_vector_type(4)));

namespace {

constexpr int XA = 10;
constexpr int NLF = 10;
constexpr int NA = 14;
constexpr int W3A = 8;
constexpr int W1A = 16;
constexpr int PDELAY = 14, PDELAY_O = 20, PSLEEP = 1;

constexpr u32 WS_EPOCH = 0, WS_TMO = 128, WS_FLAGS = 4096;
constexpr u32 FL_X = 0, FL_KV = 64, FL_O = 96, FL_XA = 128, FL_H = 192, FL_X1 = 256, FL_H2 = 320, FL_PT = 384;
constexpr u32 SZ_ACT = 2048u * 512u * 2u, SZ_ST = 64u * 4096u, SZ_KVP = 256u * 2176u, SZ_H2 = 2048u * 1792u * 2u;
constexpr u32 O_X = 0, O_XST = O_X + SZ_ACT, O_KVP = O_XST + SZ_ST, O_O = O_KVP + SZ_KVP, O_XA = O_O + SZ_ACT,
              O_XAST = O_XA + SZ_ACT, O_H = O_XAST + SZ_ST, O_X1 = O_H + SZ_ACT, O_X1ST = O_X1 + SZ_ACT,
              O_H2 = O_X1ST + SZ_ST, SZ_LAYER = O_H2 + SZ_H2;
constexpr u32 WS_L0 = 65536, WS_W3T = WS_L0 + 2 * SZ_LAYER, SZ_WT = 512u * 1792u * 2u, WS_W4T = WS_W3T + SZ_WT,
              WS_W1P = WS_W4T + SZ_WT, WS_END = WS_W1P + 16u * 98304u;
static_assert(SZ_LAYER % 128 == 0, "align");

constexpr u32 L_CS1 = 0;
constexpr u32 L_CS3 = 512;
constexpr u32 L_CS5 = 1024;
constexpr u32 L_STM = 2048;
constexpr u32 L_STR = 2304;
constexpr u32 L_WPHI = 2560;
constexpr u32 L_KVT = 4608;
constexpr u32 L_ZT = 6656;
constexpr u32 L_CNT = 6912;
constexpr u32 L_DUMMY = 7168;
constexpr u32 L_W = 8192;
constexpr u32 L_A = 98304;
constexpr u32 L_PK = 106496;
constexpr u32 L_V = 122880;
constexpr u32 L_PT = 131072;
constexpr u32 L_RED = 114688;
constexpr u32 LDS_BYTES = 163840;

constexpr float LN_EPS = 1e-5f;
constexpr float LOG2E = 1.4426950408889634f;
constexpr u32 SPIN_CAP = 1u << 17;
constexpr u32 ONES = 0x3f803f80u;

struct Params {
  const unsigned short *x, *wqkv, *wphi, *wo, *w1, *w2, *w3, *w4;
  unsigned short* out;
  unsigned char* ws;
};

template <int N, class F> DI void static_for(F&& f) {
  if constexpr (N > 0) { static_for<N - 1>(f); f(std::integral_constant<int, N - 1>{}); }
}
DI u32 opq_s(u32 x) { asm volatile("" : "+s"(x)); return x; }
DI u32 opq_v(u32 x) { asm volatile("" : "+v"(x)); return x; }
DI int fresh_lane() {
  int l;
  asm volatile("v_mbcnt_lo_u32_b32 %0, -1, 0\n\tv_mbcnt_hi_u32_b32 %0, -1, %0" : "=v"(l));
  return l;
}
DI u32 pk2(float a, float b) {
  f32x2 v = {a, b};
  return __builtin_bit_cast(u32, __builtin_convertvector(v, bf16x2));
}
DI float as_f(u32 x) { return __builtin_bit_cast(float, x); }
DI u32 as_u(float x) { return __builtin_bit_cast(u32, x); }
DI float bflo(u32 u) { return __builtin_bit_cast(float, u << 16); }
DI float bfhi(u32 u) { return __builtin_bit_cast(float, u & 0xffff0000u); }
DI f32x16 mfma32(u32x4 a, u32x4 b, f32x16 c) {
  return __builtin_amdgcn_mfma_f32_32x32x16_bf16(__builtin_bit_cast(bf16x8, a), __builtin_bit_cast(bf16x8, b), c, 0, 0, 0);
}
DI f32x4 mfma16(u32x4 a, u32x4 b, f32x4 c) {
  return __builtin_amdgcn_mfma_f32_16x16x32_bf16(__builtin_bit_cast(bf16x8, a), __builtin_bit_cast(bf16x8, b), c, 0, 0, 0);
}
DI u32x4 lds128(u32 a) { return *(LAS u32x4*)a; }
DI f32x4 lds128f(u32 a) { return *(LAS f32x4*)a; }
DI float ldsf(u32 a) { return *(LAS float*)a; }
DI void stsf(u32 a, float v) { *(LAS float*)a = v; }
DI void sts128(u32 a, u32x4 v) { *(LAS u32x4*)a = v; }
DI void sts128f(u32 a, f32x4 v) { *(LAS f32x4*)a = v; }
DI u32x4 trfrag(u32 a0, u32 a1) {
  s16x4 lo = __builtin_amdgcn_ds_read_tr16_b64_v4i16((LAS s16x4*)a0);
  s16x4 hi = __builtin_amdgcn_ds_read_tr16_b64_v4i16((LAS s16x4*)a1);
  return __builtin_bit_cast(u32x4, __builtin_shufflevector(lo, hi, 0, 1, 2, 3, 4, 5, 6, 7));
}
DI u32x4 mk_srd(const void* p, u32 bytes) {
  const u64 a = (u64)p;
  return u32x4{(u32)a, (u32)(a >> 32) & 0xffffu, bytes, 0x00020000u};
}
template <u32 SC, u32 LC> DI void dma_sc1(u32x4 srd, u32 voff, u32 sb, u32 lb) {
  u32 t;
  asm volatile("s_add_u32 m0, %1, %2\n\ts_add_u32 %0, %3, %4\n\tbuffer_load_dwordx4 %5, %6, %0 offen sc1 lds"
               : "=&s"(t) : "s"(lb), "i"(LC), "s"(sb), "i"(SC), "v"(voff), "s"(srd) : "memory", "scc");
}
template <u32 SC, u32 LC> DI void dma_pl(u32x4 srd, u32 voff, u32 sb, u32 lb) {
  u32 t;
  asm volatile("s_add_u32 m0, %1, %2\n\ts_add_u32 %0, %3, %4\n\tbuffer_load_dwordx4 %5, %6, %0 offen lds"
               : "=&s"(t) : "s"(lb), "i"(LC), "s"(sb), "i"(SC), "v"(voff), "s"(srd) : "memory", "scc");
}
template <int N> DI void wait_bar() { asm volatile("s_waitcnt vmcnt(%0) lgkmcnt(0)\n\ts_barrier" ::"i"(N) : "memory"); }
template <int N> DI void wait_vm() { asm volatile("s_waitcnt vmcnt(%0)" ::"i"(N) : "memory"); }
DI void bar_lds() { asm volatile("s_waitcnt lgkmcnt(0)\n\ts_barrier" ::: "memory"); }
DI float xhalf_sum(float v) {
  auto r = __builtin_amdgcn_permlane32_swap(as_u(v), as_u(v), false, false);
  return as_f(r[0]) + as_f(r[1]);
}
DI f32x2 pr(const f32x16& v, int i) { return f32x2{v[2 * i], v[2 * i + 1]}; }
DI void setpr(f32x16& v, int i, f32x2 x) { v[2 * i] = x[0]; v[2 * i + 1] = x[1]; }
DI f32x2 bc2(float a) { return f32x2{a, a}; }
DI f32x2 fma2(f32x2 a, f32x2 b, f32x2 c) { return __builtin_elementwise_fma(a, b, c); }
DI void ln_fold(f32x16& v, float s, float ms, const f32x16& cs) {
#pragma unroll
  for (int i = 0; i < 8; ++i) setpr(v, i, fma2(pr(v, i), bc2(s), pr(cs, i) * bc2(ms)));
}
DI void gelu16(f32x16& x) {
  const float c1 = -2.3022081986f, c2 = -0.1029432397f;
  f32x16 t;
#pragma unroll
  for (int i = 0; i < 8; ++i) { const f32x2 a = pr(x, i); setpr(t, i, fma2(a * a, bc2(c2), bc2(c1))); }
  SB();
#pragma unroll
  for (int i = 0; i < 8; ++i) setpr(t, i, pr(t, i) * pr(x, i));
  SB();
#pragma unroll
  for (int i = 0; i < 16; ++i) t[i] = __builtin_amdgcn_exp2f(t[i]);
  SB();
#pragma unroll
  for (int i = 0; i < 8; ++i) setpr(t, i, pr(t, i) + bc2(1.0f));
  SB();
#pragma unroll
  for (int i = 0; i < 16; ++i) t[i] = __builtin_amdgcn_rcpf(t[i]);
  SB();
#pragma unroll
  for (int i = 0; i < 8; ++i) setpr(x, i, pr(x, i) * pr(t, i));
  SB();
}
DI float sum16(const f32x16& v) {
  f32x2 a = pr(v, 0) + pr(v, 1), b = pr(v, 2) + pr(v, 3), c = pr(v, 4) + pr(v, 5), d = pr(v, 6) + pr(v, 7);
  a += b; c += d; a += c;
  return a[0] + a[1];
}
DI float sumsq16(const f32x16& v) {
  f32x2 a = pr(v, 0) * pr(v, 0), b = pr(v, 1) * pr(v, 1), c = pr(v, 2) * pr(v, 2), d = pr(v, 3) * pr(v, 3);
  a = fma2(pr(v, 4), pr(v, 4), a); b = fma2(pr(v, 5), pr(v, 5), b); c = fma2(pr(v, 6), pr(v, 6), c); d = fma2(pr(v, 7), pr(v, 7), d);
  a += b; c += d; a += c;
  return a[0] + a[1];
}

struct Ctx {
  __amdgpu_buffer_rsrc_t rs;
  u32x4 srd;
  GAS unsigned char* ws;
  u64 tag;
  int w, g, j, b;
};

template <int DELAY> DI void poll(const Ctx& c, u32 line_of_lane, u32 slot, bool active) {
  const GAS u64* p = (const GAS u64*)(c.ws + WS_FLAGS + line_of_lane * 128u + slot * 8u);
  GAS u32* tmo = (GAS u32*)(c.ws + WS_TMO);
  __builtin_amdgcn_s_sleep(DELAY);
  for (u32 spins = 1;; ++spins) {
    u64 v = c.tag;
    if (active) v = __hip_atomic_load(p, RLX_AGENT);
    if (__all(v == c.tag)) break;
    __builtin_amdgcn_s_sleep(PSLEEP);
    if ((spins & 63u) == 0u) {
      if (__builtin_amdgcn_readfirstlane(__hip_atomic_load(tmo, RLX_AGENT)) == (u32)c.tag) break;
      if (spins > SPIN_CAP) { __hip_atomic_store(tmo, (u32)c.tag, RLX_AGENT); break; }
    }
  }
  __builtin_amdgcn_fence(__ATOMIC_ACQUIRE, "wavefront");
}
DI void publish(const Ctx& c, int cnt, u32 nwaves, u32 line, u32 slot) {
  asm volatile("s_waitcnt vmcnt(0)" ::: "memory");
  if (fresh_lane() == 0) {
    const u32 old = __hip_atomic_fetch_add((LAS u32*)(L_CNT + cnt * 4), 1u, __ATOMIC_RELAXED, __HIP_MEMORY_SCOPE_WORKGROUP);
    if (old == nwaves - 1) {
      u64 tg = c.tag;
      asm volatile("" : "+s"(tg));
      __hip_atomic_store((GAS u64*)(c.ws + WS_FLAGS + line * 128u + slot * 8u), tg, RLX_AGENT);
    }
  }
}
DI void lds_arrive(int cnt) {
  if (fresh_lane() == 0) __hip_atomic_fetch_add((LAS u32*)(L_CNT + cnt * 4), 1u, __ATOMIC_RELAXED, __HIP_MEMORY_SCOPE_WORKGROUP);
}
DI void wait_cnt(int cnt, u32 n) {
  for (u32 spins = 0; spins < (1u << 24); ++spins) {
    const u32 v = __builtin_amdgcn_readfirstlane(__hip_atomic_load((LAS u32*)(L_CNT + cnt * 4), __ATOMIC_RELAXED, __HIP_MEMORY_SCOPE_WORKGROUP));
    if (v >= n) break;
    __builtin_amdgcn_s_sleep(1);
  }
  asm volatile("" ::: "memory");
}
DI void st128(const Ctx& c, u32x4 v, u32 voff, u32 soff) { __builtin_amdgcn_raw_buffer_store_b128(v, c.rs, voff, soff, 16); }
DI u32x4 ld128(const Ctx& c, u32 voff, u32 soff) { return __builtin_amdgcn_raw_buffer_load_b128(c.rs, voff, soff, 16); }

template <int N> DI void warm(const Ctx& c, int lane, u32 soff) {
  const u32 sb = opq_s(soff);
  static_for<N>([&](auto I) { constexpr int i = I; dma_sc1<i * 1024, 0>(c.srd, lane * 16, sb, L_DUMMY); });
}
DI void warm_stats(const Ctx& c, int lane, u32 soff) {
  if (lane < 16) dma_sc1<0, 0>(c.srd, lane * 16, soff, L_DUMMY);
}
DI void pack2(const f32x16& v, u32x4& f0, u32x4& f1) {
  f0 = u32x4{pk2(v[0], v[1]), pk2(v[2], v[3]), pk2(v[4], v[5]), pk2(v[6], v[7])};
  f1 = u32x4{pk2(v[8], v[9]), pk2(v[10], v[11]), pk2(v[12], v[13]), pk2(v[14], v[15])};
}
DI void publish_tile(const Ctx& c, int lane, const f32x16& v, u32 soff) {
  u32x4 f0, f1;
  pack2(v, f0, f1);
  st128(c, f0, lane * 16, soff);
  st128(c, f1, lane * 16 + 1024, soff);
}
DI void publish_stats(const Ctx& c, int lane, const f32x16& v, u32 soff) {
  float s = sum16(v), q = sumsq16(v);
  s = xhalf_sum(s); q = xhalf_sum(q);
  const u32x2 o = {as_u(s), as_u(q)};
  if (lane < 32) __builtin_amdgcn_raw_buffer_store_b64(o, c.rs, lane * 8, soff, 16);
}
DI u32x2 sum_bf16x4(const u32x2 (&v)[16]) {
  f32x2 a = {0.f, 0.f}, b = {0.f, 0.f};
#pragma unroll
  for (int r = 0; r < 16; ++r) {
    const u32 x = v[r].x, y = v[r].y;
    a += f32x2{bflo(x), bfhi(x)};
    b += f32x2{bflo(y), bfhi(y)};
  }
  return u32x2{pk2(a[0], a[1]), pk2(b[0], b[1])};
}
struct Stat8 { u32x2 v[8]; };
DI void load_stats(const Ctx& c, int lane, Stat8& st, u32 soff) {
  const u32 vo = (lane >> 5) * 2048 + (lane & 31) * 8;
#pragma unroll
  for (int i = 0; i < 8; ++i) st.v[i] = __builtin_amdgcn_raw_buffer_load_b64(c.rs, vo + i * 256, soff, 16);
}
DI void load_stats_asm(const Ctx& c, int lane, Stat8& st, u32 soff) {
  const u32 vo = (lane >> 5) * 2048 + (lane & 31) * 8;
#define LS1(i) asm volatile("buffer_load_dwordx2 %0, %1, %2, %3 offen offset:%4 sc1" : "=v"(st.v[i]) : "v"(vo), "s"(c.srd), "s"(soff), "i"(i * 256) : "memory")
  LS1(0); LS1(1); LS1(2); LS1(3); LS1(4); LS1(5); LS1(6); LS1(7);
#undef LS1
}
DI void landed_stats(Stat8& st) {
  asm volatile("" : "+v"(st.v[0]), "+v"(st.v[1]), "+v"(st.v[2]), "+v"(st.v[3]), "+v"(st.v[4]), "+v"(st.v[5]), "+v"(st.v[6]), "+v"(st.v[7]));
}
DI void finish_stats(const Stat8& st, float& mean, float& var) {
  f32x2 a[8];
#pragma unroll
  for (int i = 0; i < 8; ++i) { const u32 x = st.v[i].x, y = st.v[i].y; a[i] = f32x2{as_f(x), as_f(y)}; }
  a[0] += a[1]; a[2] += a[3]; a[4] += a[5]; a[6] += a[7]; a[0] += a[2]; a[4] += a[6]; a[0] += a[4];
  const float s = xhalf_sum(a[0][0]), q = xhalf_sum(a[0][1]);
  mean = s * (1.0f / 512.0f);
  var = __builtin_fmaxf(__builtin_fmaf(-mean, mean, q * (1.0f / 512.0f)), 0.f);
}
DI f32x16 load_cs(u32 base, int h) {
  f32x16 r;
#pragma unroll
  for (int q = 0; q < 4; ++q) {
    const f32x4 t = lds128f(base + h * 16 + 32 * q);
    r[4 * q] = t[0]; r[4 * q + 1] = t[1]; r[4 * q + 2] = t[2]; r[4 * q + 3] = t[3];
  }
  return r;
}
DI void store_cs(u32 base, const f32x16& v, int lane) {
  if ((lane & 31) == 0) {
#pragma unroll
    for (int q = 0; q < 4; ++q) sts128f(base + (lane >> 5) * 16 + 32 * q, f32x4{v[4 * q], v[4 * q + 1], v[4 * q + 2], v[4 * q + 3]});
  }
}

DI void colsum_R(const u32x4 (&R)[32], u32 dst, int lane) {
  f32x16 a = {};
  const u32x4 ones = {ONES, ONES, ONES, ONES};
#pragma unroll
  for (int ks = 0; ks < 32; ++ks) a = mfma32(R[ks], ones, a);
  store_cs(dst, a, lane);
}


template <int LO, int HI> DI void dma_w_small(const Ctx& c, const unsigned short* wp) {
  const int lane = fresh_lane();
  const u32x4 srd = mk_srd(wp, 512u * 512u * 2u);
  const int r = lane >> 3;
  const u32 voff = r * 1024 + (((lane & 7) ^ ((r & 2) << 1)) * 16) + 128 * c.j;
  const u32 sb = opq_s((c.w - 4) * 16 * 8192), lb = opq_s(L_W + (c.w - 4) * 16 * 1024);
  static_for<HI - LO>([&](auto I) { constexpr int i = LO + I; dma_pl<i * 8192, i * 1024>(srd, voff, sb, lb); });
}
template <int LO, int HI> DI void dma_w1p(const Ctx& c, int hw) {
  const int lane = fresh_lane();
  const u32 sb = opq_s(WS_W1P + (c.j + 8 * (c.g & 1)) * 98304 + hw * 8192), lb = opq_s(L_W + hw * 8192);
  static_for<HI - LO>([&](auto I) {
    constexpr int i = LO + I, sec = i / 8, n = i % 8;
    dma_sc1<sec * 32768 + n * 1024, sec * 32768 + n * 1024>(c.srd, lane * 16, sb, lb);
  });
}
DI void stage_w_small(const Ctx& c, const unsigned short* wp, u32x4 (&WS)[16]) {
  const int lane = fresh_lane();
  const __amdgpu_buffer_rsrc_t rs = __builtin_amdgcn_make_buffer_rsrc((void*)wp, 0, 512u * 512u * 2u, 0x00020000);
  const int r = lane >> 3;
  const u32 voff = r * 1024 + (((lane & 7) ^ ((r & 2) << 1)) * 16) + 128 * c.j;
  const u32 sb = opq_s((c.w - 4) * 16 * 8192);
#pragma unroll
  for (int i = 0; i < 16; ++i) {
    WS[i] = __builtin_amdgcn_raw_buffer_load_b128(rs, voff, sb + i * 8192, 0);
  }
  SB();
}
DI void unstage_w_small(const Ctx& c, const u32x4 (&WS)[16]) {
  const u32 a = opq_v(L_W + (c.w - 4) * 16 * 1024 + fresh_lane() * 16);
#pragma unroll
  for (int i = 0; i < 16; ++i) sts128(a + i * 1024, WS[i]);
}
template <int LO, int HI> DI void dma_w1(const Ctx& c, const unsigned short* wp, int hw) {
  const int lane = fresh_lane();
  const u32x4 srd = mk_srd(wp, 512u * 1536u * 2u);
  const u32 voff = (lane >> 2) * 3072 + (lane & 3) * 16 + 64 * (c.j + 8 * (c.g & 1));
  const int rot = 2 * (((c.g >> 1) & 1) * 2 + (c.j & 1));
  static_for<HI - LO>([&](auto I) {
    constexpr int i = LO + I, sec = i % 3, n0 = i / 3;
    const int n = (n0 + rot) & 7;
    dma_pl<sec * 1024, sec * 32768>(srd, voff, opq_s((hw * 8 + n) * 49152), opq_s(L_W + (hw * 8 + n) * 1024));
  });
}

template <bool SHIFT, int Q>
DI void small_issue(const Ctx& c, int lane, u32 sb, u32 lb) {
  constexpr int q = Q, d = q == 0 ? -1 : q == 1 ? 1 : q == 2 ? -2 : 2;
  static_for<2>([&](auto T) {
    constexpr int t = T;
    if constexpr (SHIFT) {
      const int tau = 64 * c.g + 32 * t + (lane & 31) + d;
      const u32 vo = (tau < 0 || tau >= 2048) ? 0x80000000u : (u32)((tau >> 5) * 32768 + ((tau & 31) + (lane & 32)) * 16);
      dma_sc1<q * 8192, q * 16384 + t * 1024>(c.srd, vo, sb, lb);
      dma_sc1<q * 8192 + 1024, q * 16384 + 2048 + t * 1024>(c.srd, vo, sb, lb);
    } else {
      dma_sc1<t * 32768 + q * 8192, q * 16384 + t * 1024>(c.srd, lane * 16, sb, lb);
      dma_sc1<t * 32768 + q * 8192 + 1024, q * 16384 + 2048 + t * 1024>(c.srd, lane * 16, sb, lb);
    }
  });
}
template <bool SHIFT, int EX, class E, class F>
DI void small_feed(const Ctx& c, int lane, u32 abase, E&& extra, F&& between) {
  const int hw = c.w - 4;
  const u32 sb = opq_s(abase + (SHIFT ? 0 : 2 * c.g * 32768) + hw * 2048), lb = opq_s(L_A + hw * 4096);
  small_issue<SHIFT, 0>(c, lane, sb, lb);
  small_issue<SHIFT, 1>(c, lane, sb, lb);
  extra();
  wait_bar<4 + EX>();
  small_issue<SHIFT, 2>(c, lane, sb, lb);
  between(std::integral_constant<int, 0>{});
  wait_bar<4 + EX>();
  small_issue<SHIFT, 3>(c, lane, sb, lb);
  between(std::integral_constant<int, 1>{});
  wait_bar<4>();
  between(std::integral_constant<int, 2>{});
  wait_bar<0>();
  between(std::integral_constant<int, 3>{});
}
DI u32 small_wb(int lane, int cc) {
  const int gq = lane >> 4, q4 = (lane & 15) >> 2, p = lane & 3;
  return L_W + (4 * (gq >> 1) + q4) * 128 + (((4 * cc + 2 * (gq & 1) + (p >> 1)) ^ ((q4 & 2) << 1)) * 16) + 8 * (p & 1);
}
DI f32x16 small_compute(const Ctx& c, int lane) {
  const int w = c.w;
  const u32 wb = opq_v(small_wb(lane, w & 1));
  const u32 ab = opq_v(L_A + (w >> 1) * 1024 + lane * 16);
  f32x16 acc = {};
  u32x4 wf[32], xf[32];
  constexpr int PD = 3;
  static_for<4>([&](auto Q) {
    constexpr int q = Q;
    bar_lds();
    if constexpr (q == 0) static_for<PD>([&](auto K) { constexpr int ks = K; wf[ks] = trfrag(wb + ks * 2048, wb + ks * 2048 + 1024); });
    static_for<PD>([&](auto K) { constexpr int ks = 8 * q + K; xf[ks] = lds128(ab + ks * 2048); });
    SB();
    static_for<8>([&](auto K) {
      constexpr int kk = K, ks = 8 * q + kk;
      if constexpr (kk + PD < 8) xf[ks + PD] = lds128(ab + (ks + PD) * 2048);
      if constexpr (ks + PD < 32) wf[ks + PD] = trfrag(wb + (ks + PD) * 2048, wb + (ks + PD) * 2048 + 1024);
      SB();
      acc = mfma32(wf[ks], xf[ks], acc);
      SB();
    });
    PIN(acc);
  });
  return acc;
}
struct Epi { f32x16 t; u32x4 f0, f1; };
template <int S> DI void epi_step(f32x16& x, Epi& e, float r, float ms, const f32x4 (&cs)[4]) {
  const float c1 = -2.3022081986f, c2 = -0.1029432397f;
  if constexpr (S < 4) {
#pragma unroll
    for (int i = 0; i < 4; ++i) x[4 * S + i] = __builtin_fmaf(x[4 * S + i], r, ms * cs[S][i]);
  } else if constexpr (S < 8) {
#pragma unroll
    for (int i = 4 * (S - 4); i < 4 * (S - 4) + 4; ++i) e.t[i] = __builtin_fmaf(x[i] * x[i], c2, c1) * x[i];
  } else if constexpr (S < 14) {
#pragma unroll
    for (int i = 3 * (S - 8); i < 3 * (S - 8) + 3 && i < 16; ++i) e.t[i] = __builtin_amdgcn_exp2f(e.t[i]);
  } else if constexpr (S < 16) {
#pragma unroll
    for (int i = 8 * (S - 14); i < 8 * (S - 14) + 8; ++i) e.t[i] += 1.0f;
  } else if constexpr (S < 22) {
#pragma unroll
    for (int i = 3 * (S - 16); i < 3 * (S - 16) + 3 && i < 16; ++i) e.t[i] = __builtin_amdgcn_rcpf(e.t[i]);
  } else if constexpr (S < 25) {
#pragma unroll
    for (int i = 6 * (S - 22); i < 6 * (S - 22) + 6 && i < 16; ++i) x[i] *= e.t[i];
  } else if constexpr (S == 25) {
    e.f0 = u32x4{pk2(x[0], x[1]), pk2(x[2], x[3]), pk2(x[4], x[5]), pk2(x[6], x[7])};
  } else if constexpr (S == 26) {
    e.f1 = u32x4{pk2(x[8], x[9]), pk2(x[10], x[11]), pk2(x[12], x[13]), pk2(x[14], x[15])};
  }
}
DI float max3(float a, float b, float c) { return __builtin_fmaxf(__builtin_fmaxf(a, b), c); }
DI float min3(float a, float b, float c) { return __builtin_fminf(__builtin_fminf(a, b), c); }
DI void feature_map(const f32x16& p, u32x4 (&f)[4]) {
  const float mx = max3(max3(max3(p[0], p[1], p[2]), max3(p[3], p[4], p[5]), max3(p[6], p[7], p[8])),
                        max3(max3(p[9], p[10], p[11]), max3(p[12], p[13], p[14]), p[15]), p[15]);
  const float mn = min3(min3(min3(p[0], p[1], p[2]), min3(p[3], p[4], p[5]), min3(p[6], p[7], p[8])),
                        min3(min3(p[9], p[10], p[11]), min3(p[12], p[13], p[14]), p[15]), p[15]);
  const float a = -mx * LOG2E, bb = mn * LOG2E;
  f32x16 ep, em;
  SB();
#pragma unroll
  for (int i = 0; i < 8; ++i) { setpr(ep, i, fma2(pr(p, i), bc2(LOG2E), bc2(a))); setpr(em, i, fma2(pr(p, i), bc2(-LOG2E), bc2(bb))); }
  SB();
#pragma unroll
  for (int i = 0; i < 16; ++i) { ep[i] = __builtin_amdgcn_exp2f(ep[i]); em[i] = __builtin_amdgcn_exp2f(em[i]); }
  SB();
  const float rp = __builtin_amdgcn_rcpf(sum16(ep)), rm = __builtin_amdgcn_rcpf(sum16(em));
  SB();
#pragma unroll
  for (int i = 0; i < 8; ++i) { setpr(ep, i, pr(ep, i) * bc2(rp)); setpr(em, i, pr(em, i) * bc2(rm)); }
  SB();
  pack2(ep, f[0], f[1]);
  pack2(em, f[2], f[3]);
}

template <int LO, int HI> DI void load_R(const Ctx& c, int lane, u32x4 (&R)[32], u32 soff) {
  const u32 sb = opq_s(soff);
#pragma unroll
  for (int ks = LO; ks < HI; ++ks) R[ks] = ld128(c, lane * 16 + (ks & 3) * 1024, sb + (ks >> 2) * 4096);
  SB();
}
template <int LO, int HI> DI void undef_part(u32x4 (&R)[32]) {
#pragma unroll
  for (int i = LO; i < HI; ++i) asm volatile("" : "=v"(R[i]));
}

__global__ void __launch_bounds__(512, 2) mega(Params p) {
  Ctx c;
  c.w = __builtin_amdgcn_readfirstlane(threadIdx.x >> 6);
  c.b = blockIdx.x;
  { const int X = c.b & 7, i = c.b >> 3; c.g = X * 4 + (i >> 3); c.j = i & 7; }
  c.ws = (GAS unsigned char*)p.ws;
  c.rs = __builtin_amdgcn_make_buffer_rsrc(p.ws, 0, WS_END, 0x00020000);
  c.srd = mk_srd(p.ws, WS_END);
  c.tag = *(const GAS u64*)(c.ws + WS_EPOCH) + 1;
  const int w = c.w, g = c.g, j = c.j;
  const int G2 = g >> 1, hp = j + 8 * (g & 1);

  f32x16 xres = {};
  u32x4 R[32];
  const int myT = 2 * g + ((w >> 1) & 1), myct = 2 * j + (w & 1);

  const u32 w3base = WS_W3T + (7 * j + w) * 32768;
  {
    const int lane = fresh_lane(), tok = lane & 31, h = lane >> 5;
    const int gq = lane >> 4, q4 = (lane & 15) >> 2, pp = lane & 3;
    if (w == 7 && lane < 32) *(LAS u32*)(L_CNT + lane * 4) = 0u;
    u32x2 xin[4];
    if (w < 4) {
      const GAS unsigned short* xp = (const GAS unsigned short*)p.x + (size_t)(32 * myT + tok) * 512 + 32 * myct + 4 * h;
#pragma unroll
      for (int q = 0; q < 4; ++q) xin[q] = *(const GAS u32x2*)(xp + 8 * q);
    }
    if (w == 5) {
      const u32 off = c.b * 2048;
      const u32x4 s0 = mk_srd(p.wo, 524288u), s1 = mk_srd(p.w1, 524288u), s2 = mk_srd(p.w2, 524288u);
      dma_pl<0, 0>(s0, lane * 16, off, L_DUMMY); dma_pl<1024, 0>(s0, lane * 16, off, L_DUMMY);
      dma_pl<0, 0>(s1, lane * 16, off, L_DUMMY); dma_pl<1024, 0>(s1, lane * 16, off, L_DUMMY);
      dma_pl<0, 0>(s2, lane * 16, off, L_DUMMY); dma_pl<1024, 0>(s2, lane * 16, off, L_DUMMY);
    }
    bar_lds();
    if (w < 4) {
#pragma unroll
      for (int q = 0; q < 4; ++q) {
        xres[4 * q] = bflo(xin[q].x); xres[4 * q + 1] = bfhi(xin[q].x);
        xres[4 * q + 2] = bflo(xin[q].y); xres[4 * q + 3] = bfhi(xin[q].y);
      }
      publish_tile(c, lane, xres, WS_L0 + O_X + myT * 32768 + myct * 2048);
      publish_stats(c, lane, xres, WS_L0 + O_XST + myT * 4096 + myct * 256);
      publish(c, 0, 4, FL_X + g, j);
      warm<2>(c, lane, WS_L0 + O_X + myT * 32768 + myct * 2048);
      warm_stats(c, lane, WS_L0 + O_XST + myT * 4096 + myct * 256);
    } else {
      dma_w1<0, 24>(c, p.wqkv, w - 4);
      for (int f = w - 4; f < 14; f += 4) {
        const int F = g * 14 + f;
        const bool is3 = F < 224;
        const int F2 = is3 ? F : F - 224;
        const int ct = is3 ? 7 * j + (F2 >> 5) : 2 * j + F2 / 112, pks = is3 ? F2 & 31 : F2 % 112;
        const u32 rowb = is3 ? 3584u : 1024u;
        const u32x4 srd = mk_srd(is3 ? p.w3 : p.w4, 512u * 1792u * 2u);
        dma_pl<0, 0>(srd, (lane >> 2) * rowb + (lane & 3) * 16 + ct * 64, 16 * pks * rowb, L_PT + f * 1024);
      }
      if (w == 7) {
        const int r = lane & 31, hr = (r >> 2) & 1, f = (r & 3) + 4 * (r >> 3);
        const GAS unsigned short* wp = (const GAS unsigned short*)p.wphi + (2 * hp + h) * 256 + f;
#pragma unroll
        for (int s = 0; s < 2; ++s) {
          u32x4 fr;
#pragma unroll
          for (int d = 0; d < 4; ++d) {
            const u32 lo = wp[(8 * s + 2 * d) * 16], hi = wp[(8 * s + 2 * d + 1) * 16];
            fr[d] = hr == h ? (lo | (hi << 16)) : 0u;
          }
          sts128(L_WPHI + s * 1024 + lane * 16, fr);
        }
      }
      wait_vm<0>();
      for (int f = w - 4; f < 14; f += 4) {
        const int F = g * 14 + f;
        const bool is3 = F < 224;
        const int F2 = is3 ? F : F - 224;
        const int ct = is3 ? 7 * j + (F2 >> 5) : 2 * j + F2 / 112, pks = is3 ? F2 & 31 : F2 % 112;
        const u32 tb = L_PT + f * 1024 + (4 * (gq >> 1) + q4) * 64 + 32 * (gq & 1) + 8 * pp;
        const u32 dst = is3 ? WS_W3T + ct * 32768 + pks * 1024 : WS_W4T + ct * (112 * 1024) + pks * 1024;
        st128(c, trfrag(tb, tb + 512), lane * 16, dst);
      }
    }
  }

  for (int L = 0; L < 2; ++L) {
    const u32 LB = WS_L0 + L * SZ_LAYER;
    const int cb = 2 + L * 8;
    if (w < 4) {
      u32x4 phiq[4];
      float ln_m, ln_r;
    {
      const int lane = fresh_lane(), tok = lane & 31, h = lane >> 5;
      const int gq = lane >> 4, q4 = (lane & 15) >> 2, pp = lane & 3;
      float mean = 0.f, rstd = 0.f;
      u32 xb;
      Stat8 st;
      {
        const int T = 4 * G2 + w;
        poll<PDELAY>(c, FL_X + L * 32 + (T >> 1), lane & 7, lane < 8);
        xb = opq_s(LB + O_X + T * 32768);
        load_R<0, 2>(c, lane, R, xb);
        load_stats(c, lane, st, LB + O_XST + T * 4096);
        load_R<2, XA>(c, lane, R, xb);
        asm volatile("s_barrier" ::: "memory");
      }
      const u32 colq = 16 * (pp & 1) + 8 * (gq & 1) + 4 * (pp >> 1);
      const u32 wb = L_W + (4 * (gq >> 1) + q4) * 64 + colq * 2;
      f32x16 aq = {}, ak = {}, av = {};
      {
        const u32 wb2 = opq_v(wb + 65536);
        u32x4 fk[32], fv[32], fq[32];
        auto rd = [&](auto K) {
          constexpr int ks = K;
          fk[ks] = trfrag(wb + 32768 + ks * 1024, wb + 32768 + ks * 1024 + 512);
          fv[ks] = trfrag(wb2 + ks * 1024, wb2 + ks * 1024 + 512);
          fq[ks] = trfrag(wb + ks * 1024, wb + ks * 1024 + 512);
        };
        static_for<2>(rd);
        SB();
        static_for<32>([&](auto K) {
          constexpr int ks = K;
          ak = mfma32(fk[ks], R[ks], ak);
          SB();
          if constexpr (ks + 2 < 32) rd(std::integral_constant<int, ks + 2>{});
          if constexpr (ks + XA < 32) R[ks + XA] = ld128(c, lane * 16 + ((ks + XA) & 3) * 1024, xb + ((ks + XA) >> 2) * 4096);
          SB();
          av = mfma32(fv[ks], R[ks], av);
          aq = mfma32(fq[ks], R[ks], aq);
          SB();
          if constexpr (ks == 2) {
            float var;
            finish_stats(st, mean, var);
            rstd = __builtin_amdgcn_rsqf(var + LN_EPS);
            SB();
          }
        });
        PIN(ak); PIN(av); PIN(aq);
      }
      bar_lds();
      load_R<0, W3A>(c, lane, R, w3base);
      const float mr = -mean * rstd;
      const int row = 32 * w + tok;
      {
        const f32x16 cs = load_cs(L_CS1 + 128, h);
        ln_fold(ak, rstd, mr, cs);
        u32x4 k0, k1; pack2(ak, k0, k1);
        f32x16 pk = {};
        pk = mfma32(lds128(L_WPHI + lane * 16), k0, pk);
        pk = mfma32(lds128(L_WPHI + 1024 + lane * 16), k1, pk);
        const f32x16 csv = load_cs(L_CS1 + 256, h);
        ln_fold(av, rstd, mr, csv);
        u32x4 v0, v1; pack2(av, v0, v1);
        sts128(L_V + row * 64 + (((2 * h) ^ ((row >> 1) & 3)) * 16), v0);
        sts128(L_V + row * 64 + (((2 * h + 1) ^ ((row >> 1) & 3)) * 16), v1);
        SB();
        u32x4 f[4]; feature_map(pk, f);
#pragma unroll
        for (int i = 0; i < 4; ++i) sts128(L_PK + row * 128 + (((4 * h + i) ^ (row & 7)) * 16), f[i]);
      }
      bar_lds();
      {
        const int hh = w >> 1, sg = w & 1;
        f32x4 kv = {}, z = {};
        const u32x4 ones = {ONES, ONES, ONES, ONES};
        u32x4 fa[4], fb[4];
#pragma unroll
        for (int ks = 0; ks < 4; ++ks) {
          const int r0 = 32 * ks + 8 * gq + q4, r1 = r0 + 4;
          const u32 a0 = L_PK + r0 * 128 + (((4 * hh + 2 * sg + (pp >> 1)) ^ (r0 & 7)) * 16) + 8 * (pp & 1);
          const u32 a1 = L_PK + r1 * 128 + (((4 * hh + 2 * sg + (pp >> 1)) ^ (r1 & 7)) * 16) + 8 * (pp & 1);
          const u32 b0 = L_V + r0 * 64 + (((2 * hh + (pp >> 1)) ^ ((r0 >> 1) & 3)) * 16) + 8 * (pp & 1);
          const u32 b1 = L_V + r1 * 64 + (((2 * hh + (pp >> 1)) ^ ((r1 >> 1) & 3)) * 16) + 8 * (pp & 1);
          fa[ks] = trfrag(a0, a1); fb[ks] = trfrag(b0, b1);
        }
        SB();
#pragma unroll
        for (int ks = 0; ks < 4; ++ks) { kv = mfma16(fa[ks], fb[ks], kv); z = mfma16(fa[ks], ones, z); }
        const u32 rec = LB + O_KVP + (hp * 16 + G2) * 2176;
        __builtin_amdgcn_raw_buffer_store_b64(u32x2{pk2(kv[0], kv[1]), pk2(kv[2], kv[3])}, c.rs, lane * 8, rec + w * 512, 16);
        if ((lane & 15) == 0) __builtin_amdgcn_raw_buffer_store_b64(u32x2{pk2(z[0], z[1]), pk2(z[2], z[3])}, c.rs, gq * 8, rec + 2048 + w * 32, 16);
        publish(c, cb + 0, 4, FL_KV + L * 16 + hp, G2);
      }
      load_R<W3A, 32>(c, lane, R, w3base);
      {
        const f32x16 cs = load_cs(L_CS1, h);
        ln_fold(aq, rstd, mr, cs);
        u32x4 q0, q1; pack2(aq, q0, q1);
        f32x16 pq = {};
        pq = mfma32(lds128(L_WPHI + lane * 16), q0, pq);
        pq = mfma32(lds128(L_WPHI + 1024 + lane * 16), q1, pq);
        feature_map(pq, phiq);
      }
    }
    {
      const int lane = fresh_lane(), tok = lane & 31, h = lane >> 5, gq = lane >> 4;
      bar_lds();
      {
        const u32 sb = opq_s(LB + O_KVP + hp * 16 * 2176);
        u32x2 v[16];
#pragma unroll
        for (int r = 0; r < 16; ++r) v[r] = __builtin_amdgcn_raw_buffer_load_b64(c.rs, w * 512 + lane * 8, sb + r * 2176, 16);
        SB();
        const u32x2 o = sum_bf16x4(v);
        *(LAS u32x2*)(L_KVT + ((w >> 1) * 16 + (lane & 15)) * 64 + ((w & 1) * 16 + 4 * gq) * 2) = o;
      }
      bar_lds();
      f32x16 num = {}, den = {};
      const bool on = (tok >> 4) == h;
      u32x4 a[4], zz[4];
#pragma unroll
      for (int s = 0; s < 4; ++s) {
        a[s] = lds128(L_KVT + (h * 16 + (tok & 15)) * 64 + s * 16);
        zz[s] = lds128(L_ZT + h * 64 + s * 16);
      }
      SB();
#pragma unroll
      for (int s = 0; s < 4; ++s) {
        if (!on) { a[s] = u32x4{0, 0, 0, 0}; zz[s] = u32x4{0, 0, 0, 0}; }
        num = mfma32(a[s], phiq[s], num);
        den = mfma32(zz[s], phiq[s], den);
      }
      const float r0 = __builtin_amdgcn_rcpf(den[0] + 1e-6f), r1 = __builtin_amdgcn_rcpf(den[8] + 1e-6f);
#pragma unroll
      for (int i = 0; i < 4; ++i) { setpr(num, i, pr(num, i) * bc2(r0)); setpr(num, 4 + i, pr(num, 4 + i) * bc2(r1)); }
      publish_tile(c, lane, num, LB + O_O + (4 * G2 + w) * 32768 + hp * 2048);
      publish(c, cb + 1, 4, FL_O + L * 16 + G2, hp);
      warm<2>(c, lane, LB + O_O + (4 * G2 + w) * 32768 + hp * 2048);
    }
    {
      const int lane = fresh_lane();
      bar_lds();
      const f32x16 acc = small_compute(c, lane);
      bar_lds();
      xres += acc;
      publish_tile(c, lane, xres, LB + O_XA + myT * 32768 + myct * 2048);
      publish_stats(c, lane, xres, LB + O_XAST + myT * 4096 + myct * 256);
      publish(c, cb + 2, 4, FL_XA + L * 32 + g, j);
      warm<2>(c, lane, LB + O_XA + myT * 32768 + myct * 2048);
      warm_stats(c, lane, LB + O_XAST + myT * 4096 + myct * 256);
    }
    {
      const int lane = fresh_lane(), tok = lane & 31, h = lane >> 5;
      bar_lds();
      const f32x16 acc = small_compute(c, lane);
      bar_lds();
      const float mean = ldsf(L_STM + (w >> 1) * 128 + tok * 4), var = ldsf(L_STR + (w >> 1) * 128 + tok * 4);
      const float r1 = __builtin_amdgcn_rsqf(var + LN_EPS);
      const float r2 = __builtin_amdgcn_rsqf(var * r1 * r1 + LN_EPS);
      ln_m = mean; ln_r = r1;
      const float s = r1 * r2, ms = -mean * s;
      const f32x16 cs = load_cs(L_CS3 + (w & 1) * 128, h);
      f32x16 hv = acc;
      ln_fold(hv, s, ms, cs);
      SB();
      gelu16(hv);
      publish_tile(c, lane, hv, LB + O_H + myT * 32768 + myct * 2048);
      publish(c, cb + 3, 4, FL_H + L * 32 + g, j);
      warm<2>(c, lane, LB + O_H + myT * 32768 + myct * 2048);
      if (L == 0) colsum_R(R, L_CS5 + w * 128, lane);
    }
    {
      const int lane = fresh_lane();
      bar_lds();
      const f32x16 acc = small_compute(c, lane);
      bar_lds();
      f32x16 x1;
      const float mr = -ln_m * ln_r;
#pragma unroll
      for (int i = 0; i < 8; ++i) setpr(x1, i, fma2(pr(xres, i), bc2(ln_r), bc2(mr)) + pr(acc, i));
      publish_tile(c, lane, x1, LB + O_X1 + myT * 32768 + myct * 2048);
      publish_stats(c, lane, x1, LB + O_X1ST + myT * 4096 + myct * 256);
      xres += x1;
      publish(c, cb + 4, 4, FL_X1 + L * 32 + g, j);
      warm<2>(c, lane, LB + O_X1 + myT * 32768 + myct * 2048);
      warm_stats(c, lane, LB + O_X1ST + myT * 4096 + myct * 256);
    }
    } else {
      u32x4 WS[16];
    {
      const int lane = fresh_lane();
      if (L == 0) asm volatile("s_waitcnt lgkmcnt(0)\n\ts_barrier" ::: "memory");
      else wait_bar<0>();
      if (L == 0) {
        if (w == 6) {
          const u32 la = opq_v(L_W + G2 * 2048 + lane * 16);
          const u32 so = opq_s(WS_W1P + hp * 98304 + G2 * 2048);
          u32x4 v[6];
#pragma unroll
          for (int i = 0; i < 6; ++i) v[i] = lds128(la + (i >> 1) * 32768 + (i & 1) * 1024);
#pragma unroll
          for (int i = 0; i < 6; ++i) st128(c, v[i], lane * 16, so + (i >> 1) * 32768 + (i & 1) * 1024);
        }
        publish(c, 1, 4, FL_PT + j * 2 + (g >> 4), g & 15);
      }
      if (L == 0 && w < 7) {
        const int gq = lane >> 4, q4 = (lane & 15) >> 2, pp = lane & 3;
        const u32 colq = 16 * (pp & 1) + 8 * (gq & 1) + 4 * (pp >> 1);
        const u32 wbs = L_W + (4 * (gq >> 1) + q4) * 64 + colq * 2 + (w - 4) * 32768;
        f32x16 a = {};
        const u32x4 ones = {ONES, ONES, ONES, ONES};
#pragma unroll 1
        for (int k4 = 0; k4 < 8; ++k4) {
          u32x4 f[4];
#pragma unroll
          for (int e = 0; e < 4; ++e) f[e] = trfrag(wbs + k4 * 4096 + e * 1024, wbs + k4 * 4096 + e * 1024 + 512);
          SB();
#pragma unroll
          for (int e = 0; e < 4; ++e) a = mfma32(f[e], ones, a);
        }
        store_cs(L_CS1 + (w - 4) * 128, a, lane);
      }
      if (L == 0 && w == 7) {
        __builtin_amdgcn_s_sleep(24);
        poll<PDELAY>(c, FL_PT + j * 2 + ((lane >> 4) & 1), lane & 15, lane < 32);
      }
      bar_lds();
      if (w < 7) load_R<0, W3A>(c, lane, R, w3base); else undef_part<0, W3A>(R);
      dma_w_small<0, 8>(c, p.wo);
      bar_lds();
      dma_w_small<8, 16>(c, p.wo);
      wait_cnt(cb + 0, 4);
      if (w < 7) load_R<W3A, 32>(c, lane, R, w3base); else undef_part<W3A, 32>(R);
    }
    {
      const int lane = fresh_lane();
      if (w == 7) poll<PDELAY>(c, FL_KV + L * 16 + hp, lane & 15, lane < 16);
      bar_lds();
      if (w == 4 && lane < 16) {
        const u32 sb = opq_s(LB + O_KVP + hp * 16 * 2176 + 2048);
        u32x2 v[16];
#pragma unroll
        for (int r = 0; r < 16; ++r) v[r] = __builtin_amdgcn_raw_buffer_load_b64(c.rs, lane * 8, sb + r * 2176, 16);
        SB();
        const u32x2 o = sum_bf16x4(v);
        *(LAS u32x2*)(L_ZT + ((lane >> 3) & 1) * 64 + (((lane >> 2) & 1) * 16 + 4 * (lane & 3)) * 2) = o;
      }
      bar_lds();
      stage_w_small(c, p.w1, WS);
    }
    {
      const int lane = fresh_lane();
      if (w == 7) { wait_cnt(cb + 1, 4); poll<PDELAY_O>(c, FL_O + L * 16 + G2, lane & 15, lane < 16); }
      bar_lds();
      small_feed<false, 0>(c, lane, LB + O_O, [] {}, [](auto) {});
      bar_lds();
      unstage_w_small(c, WS);
      if (L == 0) {
        asm volatile("s_waitcnt lgkmcnt(0)" ::: "memory");
        lds_arrive(28);
        if (w < 6) {
          wait_cnt(28, 4);
          const u32 wb = opq_v(small_wb(lane, w & 1));
          const u32x4 ones = {ONES, ONES, ONES, ONES};
          f32x16 a = {};
#pragma unroll 1
          for (int k4 = 0; k4 < 4; ++k4) {
            u32x4 f[8];
#pragma unroll
            for (int e = 0; e < 8; ++e) f[e] = trfrag(wb + k4 * 16384 + e * 2048, wb + k4 * 16384 + e * 2048 + 1024);
            SB();
#pragma unroll
            for (int e = 0; e < 8; ++e) a = mfma32(f[e], ones, a);
          }
          store_cs(L_CS3 + (w & 1) * 128, a, lane);
        }
      }
    }
    {
      const int lane = fresh_lane(), tok = lane & 31;
      if (w == 7) { wait_cnt(cb + 2, 4); poll<PDELAY>(c, FL_XA + L * 32 + g, lane & 7, lane < 8); }
      bar_lds();
      if (w >= 6) {
        Stat8 st;
        const u32 so = opq_s(LB + O_XAST + (2 * g + (w - 6)) * 4096);
        small_feed<false, 8>(c, lane, LB + O_XA, [&] { load_stats_asm(c, lane, st, so); }, [&](auto Q) {
          if constexpr (Q == 2) {
            landed_stats(st);
            float mean, var;
            finish_stats(st, mean, var);
            if (lane < 32) { stsf(L_STM + (w - 6) * 128 + tok * 4, mean); stsf(L_STR + (w - 6) * 128 + tok * 4, var); }
          }
        });
      } else {
        small_feed<false, 0>(c, lane, LB + O_XA, [] {}, [](auto) {});
      }
      bar_lds();
      dma_w_small<0, 16>(c, p.w2);
    }
    {
      const int lane = fresh_lane();
      if (L == 0 && w < 7) colsum_R(R, L_CS5 + w * 128, lane);
      if (w == 7) {
        wait_cnt(cb + 3, 4);
        const int gg = g + (lane >> 3) - 1;
        const int part = lane >> 3;
        poll<PDELAY>(c, FL_H + L * 32 + gg, lane & 7, lane < 24 && gg >= 0 && gg < 32 && (part == 1 || ((lane & 2) != 0) == (part == 2)));
      }
      bar_lds();
      small_feed<true, 0>(c, lane, LB + O_H, [] {}, [](auto) {});
      bar_lds();
    }
    }

    u32x4 R4[28 - NLF];
    const u32 w4base = WS_W4T + (2 * j + (w & 1)) * (112 * 1024) + 2 * (w >> 1) * 1024;
    auto r4_load = [&](int lane, u32 sb4, auto I) {
      constexpr int i = I, f = NLF + i;
      R4[i] = ld128(c, lane * 16 + (f & 1) * 1024, sb4 + (f >> 1) * 8192);
    };
    if (w < 7) {
      const int lane = fresh_lane(), tok = lane & 31, h = lane >> 5;
      bar_lds();
      dma_sc1<0, 0>(c.srd, lane * 16, opq_s(LB + O_X1 + 2 * g * 32768 + w * 1024), opq_s(L_A + w * 1024));
      const u32 ab = opq_v(L_A + lane * 16);
      f32x16 a0 = {}, a1 = {};
      static_for<4>([&](auto Q) {
        constexpr int q = Q;
        if constexpr (q == 0) wait_bar<0>(); else bar_lds();
        u32x4 xf[8];
        static_for<4>([&](auto K) { constexpr int kk = K; xf[kk] = lds128(ab + (8 * q + kk) * 1024); });
        SB();
        static_for<8>([&](auto K) {
          constexpr int kk = K;
          if constexpr (kk + 4 < 8) xf[kk + 4] = lds128(ab + (8 * q + kk + 4) * 1024);
          SB();
          a0 = mfma32(R[8 * q + kk], xf[kk], a0);
          SB();
        });
        PIN(a0);
      });
      bar_lds();
      const u32 ob = LB + O_H2 + (2 * g) * (112 * 1024) + (7 * j + w) * 2048;
      {
        const float mean = ldsf(L_STM + tok * 4), r = ldsf(L_STR + tok * 4), ms = -mean * r;
        const u32 sb4 = opq_s(w4base);
        Epi e;
        f32x4 cs[4];
#pragma unroll
        for (int q = 0; q < 4; ++q) cs[q] = lds128f(L_CS5 + w * 128 + h * 16 + 32 * q);
        u32x4 xf[32];
        static_for<3>([&](auto K) { constexpr int ks = K; xf[ks] = lds128(ab + (32 + ks) * 1024); });
        SB();
        static_for<32>([&](auto K) {
          constexpr int ks = K;
          if constexpr (ks + 3 < 32) xf[ks + 3] = lds128(ab + (32 + ks + 3) * 1024);
          SB();
          a1 = mfma32(R[ks], xf[ks], a1);
          SB();
          if constexpr (ks >= 1 && ks <= 27) { epi_step<ks - 1>(a0, e, r, ms, cs); SB(); }
          if constexpr (ks == 28) { st128(c, e.f0, lane * 16, ob); st128(c, e.f1, lane * 16 + 1024, ob); SB(); }
          if constexpr (ks >= 2 && (ks % 2 == 0) && (ks - 2) / 2 < NA) {
            r4_load(lane, sb4, std::integral_constant<int, (ks - 2) / 2>{});
            SB();
          }
        });
        PIN(a1);
      }
      {
        const float mean = ldsf(L_STM + 128 + tok * 4), r = ldsf(L_STR + 128 + tok * 4), ms = -mean * r;
        const f32x16 cs = load_cs(L_CS5 + w * 128, h);
        ln_fold(a1, r, ms, cs);
        SB();
        gelu16(a1);
        publish_tile(c, lane, a1, ob + 112 * 1024);
      }
      publish(c, cb + 5, 7, FL_H2 + L * 32 + g, j);
    } else {
      const int lane = fresh_lane(), tok = lane & 31;
      wait_cnt(cb + 4, 4);
      poll<PDELAY>(c, FL_X1 + L * 32 + g, lane & 7, lane < 8);
      bar_lds();
      {
        const u32 sb = opq_s(LB + O_X1 + 2 * g * 32768), lb = opq_s(L_A);
        auto chunk = [&](auto Q) {
          constexpr int q = Q;
          static_for<8>([&](auto K) { constexpr int f = 8 * q + K; dma_sc1<f * 1024, f * 1024>(c.srd, lane * 16, sb, lb); });
        };
        dma_sc1<7 * 1024, 7 * 1024>(c.srd, lane * 16, sb, lb);
        chunk(std::integral_constant<int, 1>{});
        wait_bar<8>();
        chunk(std::integral_constant<int, 2>{});
        wait_bar<8>();
        chunk(std::integral_constant<int, 3>{});
        wait_bar<8>();
        static_for<4>([&](auto Q) { chunk(std::integral_constant<int, 4 + Q>{}); });
        Stat8 st, st2;
        load_stats(c, lane, st, LB + O_X1ST + (2 * g) * 4096);
        load_stats(c, lane, st2, LB + O_X1ST + (2 * g + 1) * 4096);
        SB();
        wait_bar<32 + 16>();
        float mean, var;
        finish_stats(st, mean, var);
        if (lane < 32) { stsf(L_STM + tok * 4, mean); stsf(L_STR + tok * 4, __builtin_amdgcn_rsqf(var + LN_EPS)); }
        finish_stats(st2, mean, var);
        if (lane < 32) { stsf(L_STM + 128 + tok * 4, mean); stsf(L_STR + 128 + tok * 4, __builtin_amdgcn_rsqf(var + LN_EPS)); }
      }
      wait_bar<0>();
      {
        static_for<8>([&](auto WW) {
          constexpr int ww = WW;
          const u32 sb = opq_s(WS_W4T + (2 * j + (ww & 1)) * (112 * 1024) + 2 * (ww >> 1) * 1024);
          const u32 lb = opq_s(L_W + ww * NLF * 1024);
          static_for<NLF>([&](auto I) {
            constexpr int f = I;
            dma_sc1<(f >> 1) * 8192 + (f & 1) * 1024, f * 1024>(c.srd, lane * 16, sb, lb);
          });
        });
      }
      {
        const u32 sb4 = opq_s(w4base);
        static_for<NA>([&](auto I) { r4_load(lane, sb4, I); });
        SB();
      }
    }
    {
      const int lane = fresh_lane(), tok = lane & 31, h = lane >> 5;
      if (w == 7) { wait_cnt(cb + 5, 7); poll<PDELAY>(c, FL_H2 + L * 32 + g, lane & 7, lane < 8); wait_vm<0>(); }
      bar_lds();
      const u32 sb = opq_s(LB + O_H2 + (2 * g) * (112 * 1024) + w * 1024), lb = opq_s(L_A + w * 2048);
      auto issue1 = [&](auto Q, auto T) {
        constexpr int q = Q, t = T;
        dma_sc1<t * 112 * 1024 + q * 8192, (q & 3) * 16384 + t * 1024>(c.srd, lane * 16, sb, lb);
      };
      static_for<4>([&](auto Q) { issue1(Q, std::integral_constant<int, 0>{}); issue1(Q, std::integral_constant<int, 1>{}); });
      f32x16 a0 = {}, a1 = {};
      const u32 ab = opq_v(L_A + lane * 16 + (w >> 1) * 4096);
      const u32 wl = opq_v(L_W + w * NLF * 1024 + lane * 16);
      u32x4 xf[14][4], wf[14][2];
      auto rd = [&](auto Q) {
        constexpr int q = Q;
        static_for<4>([&](auto E) { constexpr int e = E; xf[q][e] = lds128(ab + (q & 3) * 16384 + (e >> 1) * 2048 + (e & 1) * 1024); });
        static_for<2>([&](auto E) { constexpr int e = E; if constexpr (2 * q + e < NLF) wf[q][e] = lds128(wl + (2 * q + e) * 1024); });
      };
      constexpr int NREM = 28 - NLF - NA;
      auto RL = [](int q) constexpr { return q < 0 ? 0 : (NREM - 2 * q <= 0 ? 0 : NREM - 2 * q >= 2 ? 2 : 1); };
      const u32 sb4 = opq_s(w4base);
      wait_bar<6>();
      rd(std::integral_constant<int, 0>{});
      static_for<14>([&](auto Q) {
        constexpr int q = Q;
        if constexpr (q < 13) wait_bar<(q + 3 < 14 ? 4 : q + 2 < 14 ? 2 : 0) + RL(q - 3) + RL(q - 2) + RL(q - 1)>();
        if constexpr (q + 1 < 14) rd(std::integral_constant<int, q + 1>{});
        SB();
        u32x4 w0, w1;
        if constexpr (2 * q < NLF) w0 = wf[q][0]; else w0 = R4[2 * q - NLF];
        if constexpr (2 * q + 1 < NLF) w1 = wf[q][1]; else w1 = R4[2 * q + 1 - NLF];
        a0 = mfma32(w0, xf[q][0], a0);
        SB();
        if constexpr (q + 4 < 14) issue1(std::integral_constant<int, q + 4>{}, std::integral_constant<int, 0>{});
        SB();
        a1 = mfma32(w0, xf[q][1], a1);
        a0 = mfma32(w1, xf[q][2], a0);
        SB();
        if constexpr (q + 4 < 14) issue1(std::integral_constant<int, q + 4>{}, std::integral_constant<int, 1>{});
        SB();
        a1 = mfma32(w1, xf[q][3], a1);
        SB();
        static_for<RL(q)>([&](auto I) { r4_load(lane, sb4, std::integral_constant<int, NA + 2 * q + I>{}); });
        SB();
      });
      PIN(a0); PIN(a1);
      bar_lds();
      {
        const int cc = w & 1, kq = w >> 1;
        if (kq != 0) {
          const u32 sl = L_RED + ((cc * 2 + 0) * 3 + (kq - 1)) * 4096 + lane * 16;
#pragma unroll
          for (int q = 0; q < 4; ++q) sts128f(sl + q * 1024, f32x4{a0[4 * q], a0[4 * q + 1], a0[4 * q + 2], a0[4 * q + 3]});
        }
        if (kq != 1) {
          const u32 sl = L_RED + ((cc * 2 + 1) * 3 + (kq > 1 ? kq - 1 : kq)) * 4096 + lane * 16;
#pragma unroll
          for (int q = 0; q < 4; ++q) sts128f(sl + q * 1024, f32x4{a1[4 * q], a1[4 * q + 1], a1[4 * q + 2], a1[4 * q + 3]});
        }
      }
      bar_lds();
      if (w < 4) {
        const int cc = w & 1, t = w >> 1;
        f32x16 s = t == 0 ? a0 : a1;
        f32x4 v[3][4];
#pragma unroll
        for (int r = 0; r < 3; ++r)
#pragma unroll
          for (int q = 0; q < 4; ++q) v[r][q] = lds128f(L_RED + ((cc * 2 + t) * 3 + r) * 4096 + q * 1024 + lane * 16);
        SB();
#pragma unroll
        for (int r = 0; r < 3; ++r)
#pragma unroll
          for (int q = 0; q < 4; ++q) { s[4 * q] += v[r][q][0]; s[4 * q + 1] += v[r][q][1]; s[4 * q + 2] += v[r][q][2]; s[4 * q + 3] += v[r][q][3]; }
        xres += s;
        if (L == 0) {
          publish_tile(c, lane, xres, WS_L0 + SZ_LAYER + O_X + myT * 32768 + myct * 2048);
          publish_stats(c, lane, xres, WS_L0 + SZ_LAYER + O_XST + myT * 4096 + myct * 256);
          publish(c, cb + 6, 4, FL_X + 32 + g, j);
          warm<2>(c, lane, WS_L0 + SZ_LAYER + O_X + myT * 32768 + myct * 2048);
          warm_stats(c, lane, WS_L0 + SZ_LAYER + O_XST + myT * 4096 + myct * 256);
        } else {
          const __amdgpu_buffer_rsrc_t ro = __builtin_amdgcn_make_buffer_rsrc(p.out, 0, 2048u * 512u * 2u, 0x00020000);
          const u32 vo = ((32 * myT + tok) * 512 + 32 * myct + 8 * h) * 2;
#pragma unroll
          for (int qq = 0; qq < 2; ++qq) {
            u32 a0 = pk2(xres[8 * qq], xres[8 * qq + 1]), a1 = pk2(xres[8 * qq + 2], xres[8 * qq + 3]);
            u32 b0 = pk2(xres[8 * qq + 4], xres[8 * qq + 5]), b1 = pk2(xres[8 * qq + 6], xres[8 * qq + 7]);
            const auto s0 = __builtin_amdgcn_permlane32_swap(a0, b0, false, false);
            const auto s1 = __builtin_amdgcn_permlane32_swap(a1, b1, false, false);
            const u32 x0 = s0[0], y0 = s0[1], x1 = s1[0], y1 = s1[1];
            __builtin_amdgcn_raw_buffer_store_b128(u32x4{x0, x1, y0, y1}, ro, vo + 32 * qq, 0, 0);
          }
        }
      } else if (L == 0) {
        dma_w1p<0, W1A>(c, w - 4);
        wait_cnt(cb + 6, 4);
        dma_w1p<W1A, 24>(c, w - 4);
      }
    }
  }
  if (c.b == 0 && c.w == 0 && fresh_lane() == 0) *(GAS u64*)(c.ws + WS_EPOCH) = c.tag;
}

}

extern "C" void kernel_launch(void* const* d_in, const int* in_sizes, int n_in, void* d_out, int out_size, void* d_ws,
                              size_t ws_size, hipStream_t stream) {
  Params p;
  p.x = (const unsigned short*)d_in[0];
  p.wqkv = (const unsigned short*)d_in[1];
  p.wphi = (const unsigned short*)d_in[2];
  p.wo = (const unsigned short*)d_in[3];
  p.w1 = (const unsigned short*)d_in[5];
  p.w2 = (const unsigned short*)d_in[6];
  p.w3 = (const unsigned short*)d_in[7];
  p.w4 = (const unsigned short*)d_in[8];
  p.out = (unsigned short*)d_out;
  p.ws = (unsigned char*)d_ws;
  (void)hipFuncSetAttribute((const void*)mega, hipFuncAttributeMaxDynamicSharedMemorySize, LDS_BYTES);
  hipLaunchKernelGGL(mega, dim3(256), dim3(512), LDS_BYTES, stream, p);
}
```
